```python
import torch

def _softmax(x, axis=-1):
    return torch.softmax(x, dim=axis)
def _gelu(x):
    return torch.nn.functional.gelu(x, approximate="tanh")
def _silu(x):
    return torch.nn.functional.silu(x)
def _sigmoid(x):
    return torch.sigmoid(x)
def _layernorm(x, axis=-1, eps=1e-5):
    m = x.mean(dim=axis, keepdim=True)
    v = x.var(dim=axis, keepdim=True, unbiased=False)
    return (x - m) / torch.sqrt(v + eps)
def _scatter_add(tgt, idx, src, axis=0):
    return tgt.clone().index_add_(axis, idx, src)
def _segment_sum(src, seg, n):
    out = torch.zeros((n,) + src.shape[1:], dtype=src.dtype)
    return out.index_add_(0, seg, src)
def _pad(x, pw):
    flat = tuple(v for pair in reversed(pw) for v in pair)
    return torch.nn.functional.pad(x, flat)

kernel_name = 'recurrent_scan__sliding_block_cached__swiglu_ffn__decode'

def setup_inputs():

    _w = (lambda a: (lambda t: t.to(torch.bfloat16) if t.is_floating_point() else t)(torch.as_tensor(a)))
    def _A_setup():
        B, Sq, D = 2, 1, 512
        Hq, Hkv, Sk, Dh = 32, 32, 8192, 128
        assert Hq % Hkv == 0, (Hq, Hkv)
        return {
            "x": _w((lambda *s: torch.randn(*s, dtype=torch.bfloat16) * 0.05)(B, Sq, D)),
            "k_cache": _w((lambda *s: torch.randn(*s, dtype=torch.bfloat16) * 1.6)(B, Hkv, Sk, Dh)),
            "v_cache": _w((lambda *s: torch.randn(*s, dtype=torch.bfloat16) * 0.2)(B, Hkv, Sk, Dh)),
            "wq": _w((lambda *s: torch.randn(*s, dtype=torch.bfloat16) * 1.6)(D, Hq * Dh) * (D ** -0.5)),
            "wo": _w((lambda *s: torch.randn(*s, dtype=torch.bfloat16) * 0.2)(Hq * Dh, D) * ((Hq * Dh) ** -0.5)),
        }
    _A_in = _A_setup()

    def _B_setup():
        B, S, D, F = 2, 1, 512, 1376
        return {
            "x": _w((lambda *s: torch.randn(*s, dtype=torch.bfloat16) * 0.05)(B, S, D)),
            "w_gate": _w((lambda *s: torch.randn(*s, dtype=torch.bfloat16) * 0.1)(D, F)),
            "w_up": _w((lambda *s: torch.randn(*s, dtype=torch.bfloat16) * 0.1)(D, F)),
            "w_down": _w((lambda *s: torch.randn(*s, dtype=torch.bfloat16) * 0.1)(F, D)),
        }
    _B_in = _B_setup()

    return {
        "A_x": _A_in["x"],
        "A_k_cache": _A_in["k_cache"],
        "A_v_cache": _A_in["v_cache"],
        "A_wq": _A_in["wq"],
        "A_wo": _A_in["wo"],
        "B_x": _B_in["x"],
        "B_w_gate": _B_in["w_gate"],
        "B_w_up": _B_in["w_up"],
        "B_w_down": _B_in["w_down"],
    }

def reference(A_x, A_k_cache, A_v_cache, A_wq, A_wo, B_x, B_w_gate, B_w_up, B_w_down):
    def _A_ref(x, k_cache, v_cache, wq, wo):
        B, Sq, D = x.shape
        Hq, Dh = 32, 128
        Hkv, Sk = k_cache.shape[1], k_cache.shape[2]
        W = min(4096, max(256, Sk // 4))
        rep = Hq // Hkv
        q = (lambda x, *d: x.permute(*d))((x @ wq).reshape(B, Sq, Hq, Dh), 0, 2, 1, 3)
        k = (lambda x, n, axis: torch.repeat_interleave(x, n, dim=axis))(k_cache[:, :, Sk - W:, :], rep, axis=1)
        v = (lambda x, n, axis: torch.repeat_interleave(x, n, dim=axis))(v_cache[:, :, Sk - W:, :], rep, axis=1)
        scores = torch.einsum("bhqd,bhkd->bhqk", q, k) * (Dh ** -0.5)
        p = _softmax(scores, axis=-1)
        o = torch.einsum("bhqk,bhkd->bhqd", p, v)
        return (lambda x, *d: x.permute(*d))(o, 0, 2, 1, 3).reshape(B, Sq, Hq * Dh) @ wo

    def _B_ref(x, w_gate, w_up, w_down):
        g = _silu(x @ w_gate)
        u = x @ w_up
        return (g * u) @ w_down

    x = A_x
    xn = _layernorm(x, axis=-1)
    s = _A_ref(xn, A_k_cache, A_v_cache, A_wq, A_wo)
    g = _silu(_B_ref(xn, B_w_gate, B_w_up, B_w_down))
    return x + s * g
```

```cpp
#include <hip/hip_runtime.h>
#include <hip/hip_bf16.h>
#include <stdint.h>

typedef unsigned short u16;
constexpr int B = 2, D = 512, H = 32, SK = 8192, DH = 128, W = 2048, F = 1376, NSPLIT = 4;

__device__ __forceinline__ float bf2f(u16 v) { return __uint_as_float((unsigned)v << 16); }
__device__ __forceinline__ float rbf(float f) { return (float)(__bf16)f; }

__device__ void layernorm_block(const u16* x, float* xn  , float* red) {
  const int t = threadIdx.x;
  for (int b = 0; b < 2; ++b) {
    float v0 = bf2f(x[b * D + t]), v1 = bf2f(x[b * D + 256 + t]);
    float s = v0 + v1;
    for (int o = 32; o > 0; o >>= 1) s += __shfl_xor(s, o);
    if ((t & 63) == 0) red[t >> 6] = s;
    __syncthreads();
    float mean = (red[0] + red[1] + red[2] + red[3]) * (1.0f / D);
    __syncthreads();
    float d0 = v0 - mean, d1 = v1 - mean;
    float q = d0 * d0 + d1 * d1;
    for (int o = 32; o > 0; o >>= 1) q += __shfl_xor(q, o);
    if ((t & 63) == 0) red[t >> 6] = q;
    __syncthreads();
    float var = (red[0] + red[1] + red[2] + red[3]) * (1.0f / D);
    __syncthreads();
    float mb = rbf(mean), vb = rbf(var);
    float den = rbf(sqrtf(rbf(vb + 1e-5f)));
    xn[b * D + t] = rbf(rbf(v0 - mb) / den);
    xn[b * D + 256 + t] = rbf(rbf(v1 - mb) / den);
  }
  __syncthreads();
}

__global__ void __launch_bounds__(256) k_prep(const u16* x, const u16* wq, const u16* wg, const u16* wu, float* q, float* hbuf) {
  __shared__ float xn[2 * D];
  __shared__ float red[4];
  layernorm_block(x, xn, red);
  const int t = threadIdx.x;
  if (blockIdx.x < 16) {
    int c = blockIdx.x * 256 + t;
    float a0 = 0, a1 = 0;
    for (int k = 0; k < D; ++k) { float w = bf2f(wq[(size_t)k * 4096 + c]); a0 += xn[k] * w; a1 += xn[D + k] * w; }
    q[c] = rbf(a0); q[4096 + c] = rbf(a1);
  } else {
    int c = (blockIdx.x - 16) * 256 + t;
    if (c < F) {
      float g0 = 0, g1 = 0, u0 = 0, u1 = 0;
      for (int k = 0; k < D; ++k) {
        float a = bf2f(wg[(size_t)k * F + c]), bb = bf2f(wu[(size_t)k * F + c]);
        g0 += xn[k] * a; g1 += xn[D + k] * a; u0 += xn[k] * bb; u1 += xn[D + k] * bb;
      }
      g0 = rbf(g0); g1 = rbf(g1); u0 = rbf(u0); u1 = rbf(u1);
      g0 = rbf(g0 / (1.f + __expf(-g0))); g1 = rbf(g1 / (1.f + __expf(-g1)));
      hbuf[c] = rbf(g0 * u0); hbuf[F + c] = rbf(g1 * u1);
    }
  }
}

__global__ void __launch_bounds__(256) k_attn(const u16* kc, const u16* vc, const float* q, float* po, float* pml) {
  __shared__ float sq[DH];
  __shared__ float sc[512];
  __shared__ float red[4];
  __shared__ float oacc[2][DH];
  const int s = blockIdx.x, h = blockIdx.y, b = blockIdx.z, t = threadIdx.x;
  if (t < DH) sq[t] = q[b * 4096 + h * DH + t];
  __syncthreads();
  const size_t base = ((size_t)(b * H + h) * SK + (SK - W) + s * 512) * DH;
  float mx = -INFINITY;
  for (int j = t; j < 512; j += 256) {
    const u16* kr = kc + base + (size_t)j * DH;
    float a = 0;
    for (int d = 0; d < DH; ++d) a += sq[d] * bf2f(kr[d]);
    a = rbf(rbf(a) * 0.08838834764831845f);
    sc[j] = a; mx = fmaxf(mx, a);
  }
  for (int o = 32; o > 0; o >>= 1) mx = fmaxf(mx, __shfl_xor(mx, o));
  if ((t & 63) == 0) red[t >> 6] = mx;
  __syncthreads();
  mx = fmaxf(fmaxf(red[0], red[1]), fmaxf(red[2], red[3]));
  __syncthreads();
  float l = 0;
  for (int j = t; j < 512; j += 256) { float p = __expf(sc[j] - mx); sc[j] = p; l += p; }
  for (int o = 32; o > 0; o >>= 1) l += __shfl_xor(l, o);
  if ((t & 63) == 0) red[t >> 6] = l;
  __syncthreads();
  l = red[0] + red[1] + red[2] + red[3];
  const int d = t & 127, half = t >> 7;
  float a = 0;
  for (int j = half * 256; j < half * 256 + 256; ++j) a += sc[j] * bf2f(vc[base + (size_t)j * DH + d]);
  oacc[half][d] = a;
  __syncthreads();
  const int slot = (b * H + h) * NSPLIT + s;
  if (t < DH) po[slot * DH + t] = oacc[0][t] + oacc[1][t];
  if (t == 0) { pml[slot * 2] = mx; pml[slot * 2 + 1] = l; }
}

__global__ void __launch_bounds__(256) k_final(const u16* x, const u16* wo, const u16* wd, const float* po, const float* pml, const float* hbuf, __hip_bfloat16* out) {
  __shared__ float o[4096];
  __shared__ float r1[4][64], r2[4][64];
  const int b = blockIdx.x >> 3, cg = blockIdx.x & 7, t = threadIdx.x;
  for (int i = t; i < 4096; i += 256) {
    int h = i >> 7, d = i & 127;
    int slot = (b * H + h) * NSPLIT;
    float M = -INFINITY;
    for (int s = 0; s < NSPLIT; ++s) M = fmaxf(M, pml[(slot + s) * 2]);
    float L = 0, a = 0;
    for (int s = 0; s < NSPLIT; ++s) { float w = __expf(pml[(slot + s) * 2] - M); L += w * pml[(slot + s) * 2 + 1]; a += w * po[(slot + s) * DH + d]; }
    o[i] = rbf(a / L);
  }
  __syncthreads();
  const int c = cg * 64 + (t & 63), kg = t >> 6;
  float a = 0;
  for (int k = kg * 1024; k < kg * 1024 + 1024; ++k) a += o[k] * bf2f(wo[(size_t)k * D + c]);
  float f = 0;
  for (int k = kg * 344; k < kg * 344 + 344; ++k) f += hbuf[b * F + k] * bf2f(wd[(size_t)k * D + c]);
  r1[kg][t & 63] = a; r2[kg][t & 63] = f;
  __syncthreads();
  if (t < 64) {
    float s = rbf(r1[0][t] + r1[1][t] + r1[2][t] + r1[3][t]);
    float g = rbf(r2[0][t] + r2[1][t] + r2[2][t] + r2[3][t]);
    g = rbf(g / (1.f + __expf(-g)));
    float r = bf2f(x[b * D + c]) + rbf(s * g);
    out[b * D + c] = __float2bfloat16(r);
  }
}

extern "C" void kernel_launch(void* const* d_in, const int* in_sizes, int n_in,
                              void* d_out, int out_size, void* d_ws, size_t ws_size,
                              hipStream_t stream) {
  const u16* x = (const u16*)d_in[0];
  const u16* kc = (const u16*)d_in[1];
  const u16* vc = (const u16*)d_in[2];
  const u16* wq = (const u16*)d_in[3];
  const u16* wo = (const u16*)d_in[4];
  const u16* wg = (const u16*)d_in[6];
  const u16* wu = (const u16*)d_in[7];
  const u16* wd = (const u16*)d_in[8];
  float* ws = (float*)d_ws;
  float* q = ws;
  float* hbuf = ws + 8192;
  float* po = ws + 16384;
  float* pml = po + 256 * 128;
  k_prep<<<22, 256, 0, stream>>>(x, wq, wg, wu, q, hbuf);
  k_attn<<<dim3(NSPLIT, H, B), 256, 0, stream>>>(kc, vc, q, po, pml);
  k_final<<<16, 256, 0, stream>>>(x, wo, wd, po, pml, hbuf, (__hip_bfloat16*)d_out);
}
```

```cpp
#include <hip/hip_runtime.h>
#include <hip/hip_bf16.h>
#include <stdint.h>

typedef unsigned u32x4 __attribute__((ext_vector_type(4)));
typedef unsigned u32x2 __attribute__((ext_vector_type(2)));
typedef float f32x2 __attribute__((ext_vector_type(2)));
typedef float f32x4 __attribute__((ext_vector_type(4)));
typedef __bf16 bf16x2 __attribute__((ext_vector_type(2)));
#define LAS __attribute__((address_space(3)))
typedef __amdgpu_buffer_rsrc_t rsrc_t;

#define PACE_Q8 1000
#define PACE_BURST 100
#define STAMPS 0

#define PMASK 0
#define GMASK 0
#define ABL 0
#define PUT_AUX 16
#define GET_AUX 16
#define WS_SHIFT 528482304
constexpr int AUX_NT = 2, AUX_SC1 = 16;
constexpr unsigned SPIN_CAP = 1u << 15;

constexpr unsigned WS_EPOCH = 0;
constexpr unsigned WS_ML = 4096;
constexpr unsigned WS_OP = WS_ML + 64 * 16 * 16;
constexpr unsigned WS_QP = WS_OP + 256 * 64 * 16;
constexpr unsigned WS_F1 = WS_QP + 64 * 256 * 16;
constexpr unsigned WS_F2 = WS_F1 + 43 * 256 * 16;
constexpr unsigned WS_YP = WS_F2 + 64 * 48 * 8 * 16;
constexpr unsigned WS_END = WS_YP + 64 * 128 * 8 * 16;

constexpr int L_XE = 0;
constexpr int L_PB = 4096;
constexpr int L_OB = 5120;
constexpr int L_FB = 7168;
constexpr int L_FLG = 7680;
constexpr int L_END = 7744;

struct Args {
  const char* x; const char* kc; const char* vc; const char* wq; const char* wo;
  const char* wg; const char* wu; const char* wd; char* out; char* ws;
};

__device__ __forceinline__ rsrc_t mk(const void* p) { return __builtin_amdgcn_make_buffer_rsrc((void*)p, 0, 0x7fffffff, 0x00020000); }
template <int AUX> __device__ __forceinline__ u32x4 ld16(rsrc_t r, unsigned voff, unsigned soff) { return __builtin_amdgcn_raw_buffer_load_b128(r, voff, soff, AUX); }
__device__ __forceinline__ float asf(unsigned u) { return __builtin_bit_cast(float, u); }
__device__ __forceinline__ unsigned asu(float f) { return __builtin_bit_cast(unsigned, f); }
__device__ __forceinline__ float dot2(unsigned a, unsigned b, float c) {
  return __builtin_amdgcn_fdot2_f32_bf16(__builtin_bit_cast(bf16x2, a), __builtin_bit_cast(bf16x2, b), c, false);
}
__device__ __forceinline__ unsigned pk(float lo, float hi) { f32x2 v = {lo, hi}; return __builtin_bit_cast(unsigned, __builtin_convertvector(v, bf16x2)); }
__device__ __forceinline__ float rbf(float f) { return (float)(__bf16)f; }
__device__ __forceinline__ float blo(unsigned u) { return asf(u << 16); }
__device__ __forceinline__ float bhi(unsigned u) { return asf(u & 0xffff0000u); }
__device__ __forceinline__ unsigned plo(unsigned a, unsigned b) { return __builtin_amdgcn_perm(b, a, 0x05040100u); }
__device__ __forceinline__ unsigned phi(unsigned a, unsigned b) { return __builtin_amdgcn_perm(b, a, 0x07060302u); }
template <int CTRL> __device__ __forceinline__ float dpp(float x) { return asf(__builtin_amdgcn_mov_dpp(asu(x), CTRL, 0xf, 0xf, true)); }
template <int CTRL> __device__ __forceinline__ unsigned dppu(unsigned x) { return __builtin_amdgcn_mov_dpp(x, CTRL, 0xf, 0xf, true); }
constexpr int XOR1 = 0xB1, XOR2 = 0x4E, XOR7 = 0x141, ROR8 = 0x128, ROR4 = 0x124;
__device__ __forceinline__ float tr32(float a, float b) { auto s = __builtin_amdgcn_permlane32_swap(asu(a), asu(b), false, false); return asf(s[0]) + asf(s[1]); }
__device__ __forceinline__ float tr16(float a, float b) { auto s = __builtin_amdgcn_permlane16_swap(asu(a), asu(b), false, false); return asf(s[0]) + asf(s[1]); }
__device__ __forceinline__ float mx32(float a) { auto s = __builtin_amdgcn_permlane32_swap(asu(a), asu(a), false, false); return fmaxf(asf(s[0]), asf(s[1])); }
__device__ __forceinline__ float mx16(float a) { auto s = __builtin_amdgcn_permlane16_swap(asu(a), asu(a), false, false); return fmaxf(asf(s[0]), asf(s[1])); }
__device__ __forceinline__ float row_sum(float v) { v += dpp<XOR1>(v); v += dpp<XOR2>(v); v += dpp<XOR7>(v); v += dpp<ROR8>(v); return v; }
__device__ __forceinline__ float row_max(float v) { v = fmaxf(v, dpp<XOR1>(v)); v = fmaxf(v, dpp<XOR2>(v)); v = fmaxf(v, dpp<XOR7>(v)); v = fmaxf(v, dpp<ROR8>(v)); return v; }
__device__ __forceinline__ float wave_sum(float v) { v = row_sum(v); v = tr16(v, v); return tr32(v, v); }
__device__ __forceinline__ float wave_max(float v) { v = row_max(v); v = mx16(v); return mx32(v); }
__device__ __forceinline__ float silu(float g) { return g * __builtin_amdgcn_rcpf(1.0f + __builtin_amdgcn_exp2f(-1.4426950408889634f * g)); }

struct Tag { unsigned t0, t1; };
template <int HOP> __device__ __forceinline__ void put(rsrc_t ws, unsigned off, float a, float b, Tag tg) {
  u32x4 g = {asu(a), asu(b), asu(a) ^ tg.t0, asu(b) ^ tg.t1};
  if (PMASK & HOP) asm volatile("" :: "v"(g)); else __builtin_amdgcn_raw_buffer_store_b128(g, ws, off, 0, PUT_AUX);
}
__device__ __forceinline__ bool good(u32x4 g, Tag tg) { return (((g.x ^ g.z ^ tg.t0) | (g.y ^ g.w ^ tg.t1)) == 0u); }
template <int HOP, int N, int STRIDE, int SLEEP> __device__ __forceinline__ void gather(rsrc_t ws, unsigned off, Tag tg, u32x4 (&v)[N]) {
  for (unsigned spin = 0;; ++spin) {
    asm volatile("" ::: "memory");
#pragma unroll
    for (int i = 0; i < N; ++i) { if (GMASK & HOP) { v[i] = u32x4{off, off, off, off}; asm volatile("" : "+v"(v[i])); } else v[i] = ld16<GET_AUX>(ws, off, i * STRIDE); }
    bool ok = true;
#pragma unroll
    for (int i = 0; i < N; ++i) ok &= good(v[i], tg);
    if (ABL || __all(ok) || spin > SPIN_CAP) break;
    if (SLEEP) __builtin_amdgcn_s_sleep(SLEEP);
  }
}
__device__ __forceinline__ unsigned now32() { return (unsigned)__builtin_amdgcn_s_memrealtime(); }
__device__ __forceinline__ void pace(unsigned t0, int kb) {
  const int tgt = ((kb * PACE_Q8) >> 8) - PACE_BURST;
  while ((int)(now32() - t0) < tgt) __builtin_amdgcn_s_sleep(1);
  __builtin_amdgcn_sched_barrier(0);
}
__device__ __forceinline__ void wg_barrier() { asm volatile("s_waitcnt lgkmcnt(0)" ::: "memory"); __builtin_amdgcn_s_barrier(); asm volatile("" ::: "memory"); }

#define STAMP(i) do {} while (0)
#define FLUSH() do {} while (0)

__device__ __forceinline__ void pair_rows(const u32x4 (&W)[8], unsigned (&P)[4][8]) {
#pragma unroll
  for (int t = 0; t < 4; ++t)
#pragma unroll
    for (int j = 0; j < 4; ++j) { P[t][2 * j] = plo(W[2 * t][j], W[2 * t + 1][j]); P[t][2 * j + 1] = phi(W[2 * t][j], W[2 * t + 1][j]); }
}
__device__ __forceinline__ f32x2 gemv32(const unsigned (&P)[4][8], unsigned opk) {
  const unsigned o0 = dppu<0x00>(opk), o1 = dppu<0x55>(opk), o2 = dppu<0xAA>(opk), o3 = dppu<0xFF>(opk);
  float acc[8];
#pragma unroll
  for (int j = 0; j < 8; ++j) acc[j] = dot2(P[3][j], o3, dot2(P[2][j], o2, dot2(P[1][j], o1, dot2(P[0][j], o0, 0.f))));
  float u[4];
#pragma unroll
  for (int j = 0; j < 4; ++j) u[j] = tr32(acc[j], acc[j + 4]);
  f32x2 r = {tr16(u[0], u[2]), tr16(u[1], u[3])};
  return r;
}

__global__ void __launch_bounds__(512, 2) fused_decode(Args a) {
  __shared__ __attribute__((aligned(16))) unsigned char smem[L_END];
  LAS unsigned char* L = (LAS unsigned char*)smem;
  const unsigned t0 = now32();
  const unsigned bid = blockIdx.x, x = bid & 7, idx = bid >> 3;
  const unsigned lane = threadIdx.x & 63, wave = __builtin_amdgcn_readfirstlane(threadIdx.x >> 6);
  const unsigned h = x * 4 + (idx >> 3), b = (idx >> 2) & 1, s = idx & 3, bh = b * 32 + h;
  const unsigned k = wave & 3;
  const size_t kvbase = ((size_t)bh * 8192 + 6144 + s * 512) * 256;
  const unsigned v0 = ((k ^ (idx << 2)) << 10) | (lane << 4);
  unsigned long long ep;
  Tag tg;
#define EPOCH_AND_BARRIER() do { const char* wsp_ = a.ws + WS_SHIFT; asm volatile("" : "+s"(wsp_)); \
    ep = *(const __attribute__((address_space(4))) unsigned long long*)(wsp_ + WS_EPOCH) + 1ull; \
    tg.t0 = (unsigned)ep; tg.t1 = (unsigned)(ep >> 32) | 0x80000000u; \
    asm volatile("" :: "s"(tg.t0), "s"(tg.t1)); wg_barrier(); } while (0)
  const rsrc_t ws = mk(a.ws + WS_SHIFT);
  volatile LAS unsigned* flg = (volatile LAS unsigned*)(L + L_FLG);
  const unsigned rg = lane >> 4, c = lane & 15;

  if (wave < 4) {
    const rsrc_t rk = mk(a.kc + kvbase);
    u32x4 K[32];
#pragma unroll
    for (int g = 0; g < 8; ++g) {
      if (g == 1) EPOCH_AND_BARRIER();
      if (g > 0) pace(t0, 32 + (g * 4 + (int)k) * 4);
#pragma unroll
      for (int i = 4 * g; i < 4 * g + 4; ++i) K[i] = ld16<AUX_NT>(rk, v0 ^ (i << 12), 0);
      __builtin_amdgcn_sched_barrier(0);
    }
    const bool has_f2 = (bid >= 172) & (bid < 215);
    const unsigned d = bid - 172;
    u32x4 W[8];
    const unsigned woff = (rg << 13) | (k << 8) | (c << 4);
    if (has_f2) {
      const rsrc_t rd = mk(a.wd + (size_t)d * 32768);
#pragma unroll
      for (int i = 0; i < 8; ++i) W[i] = ld16<AUX_NT>(rd, woff, i << 10);
    }
    if (k == 0) STAMP(1);
    unsigned q[4];
    {
      u32x4 g[4];
      gather<1, 4, 256, 16>(ws, WS_QP + (((h * 2 + b) * 4 + rg) * 64 + c) * 16, tg, g);
#pragma unroll
      for (int i = 0; i < 4; ++i) {
        float lo = asf(g[i].x), hi = asf(g[i].y);
        lo = tr16(lo, lo); hi = tr16(hi, hi);
        lo = tr32(lo, lo); hi = tr32(hi, hi);
        q[i] = pk(lo, hi);
      }
    }
    if (k == 0) STAMP(2);
    float xs[32];
#pragma unroll
    for (int i = 0; i < 32; ++i) xs[i] = dot2(K[i].w, q[3], dot2(K[i].z, q[2], dot2(K[i].y, q[1], dot2(K[i].x, q[0], 0.f))));
#define TR_STEP(HALF, CTRL, BIT) _Pragma("unroll") for (int i = 0; i < (HALF); ++i) { \
      const float t1_ = xs[i] + dpp<CTRL>(xs[i]), t2_ = xs[i + (HALF)] + dpp<CTRL>(xs[i + (HALF)]); xs[i] = (c & (BIT)) ? t2_ : t1_; }
    TR_STEP(16, ROR8, 8) TR_STEP(8, XOR7, 4) TR_STEP(4, XOR2, 2) TR_STEP(2, XOR1, 1)
#undef TR_STEP
    const float s0 = rbf(rbf(xs[0]) * 0.08838834764831845f), s1 = rbf(rbf(xs[1]) * 0.08838834764831845f);
    const float m = wave_max(fmaxf(s0, s1));
    const float p0 = __builtin_amdgcn_exp2f((s0 - m) * 1.4426950408889634f), p1 = __builtin_amdgcn_exp2f((s1 - m) * 1.4426950408889634f);
    ((LAS unsigned*)(L + L_PB))[k * 64 + lane] = pk(p0, p1);
    asm volatile("s_waitcnt lgkmcnt(0)" ::: "memory");
    if (lane == 0) flg[k] = asu(m);
    const float l = wave_sum(p0 + p1);
    if (lane == 0) put<8>(ws, WS_ML + (bh * 16 + s * 4 + k) * 16, m, l, tg);
    if (k == 0) STAMP(3);
    const unsigned gpos = (((c >> 2) * 16) | (rg << 2) | (c & 3)) * 16;
    if (has_f2) {
      u32x4 g[4];
      gather<2, 4, 1024, 16>(ws, WS_F1 + d * 4096 + gpos, tg, g);
      unsigned P[4][8];
      pair_rows(W, P);
      float v[4][2];
#pragma unroll
      for (int i = 0; i < 4; ++i) {
        float lo = asf(g[i].x), hi = asf(g[i].y);
        lo += dpp<ROR4>(lo); hi += dpp<ROR4>(hi);
        lo += dpp<ROR8>(lo); hi += dpp<ROR8>(hi);
        v[i][0] = rbf(lo); v[i][1] = rbf(hi);
      }
#pragma unroll
      for (int bb = 0; bb < 2; ++bb) {
        const unsigned hp = pk(rbf(silu(v[bb][0])) * v[2 + bb][0], rbf(silu(v[bb][1])) * v[2 + bb][1]);
        const f32x2 r = gemv32(P, hp);
        put<4>(ws, WS_F2 + (((bb * 32 + 8 * k + (lane >> 3)) * 48 + d) * 8 + (lane & 7)) * 16, r.x, r.y, tg);
      }
    }
    float wgt;
    {
      u32x4 g[1];
      gather<8, 1, 0, 4>(ws, WS_ML + (bh * 16 + c) * 16, tg, g);
      const float mi = asf(g[0].x), li = asf(g[0].y);
      float mq = fmaxf(mi, dpp<XOR1>(mi)); mq = fmaxf(mq, dpp<XOR2>(mq));
      float M = fmaxf(mq, dpp<XOR7>(mq)); M = fmaxf(M, dpp<ROR8>(M));
      const float Ls = row_sum(li * __builtin_amdgcn_exp2f((mi - M) * 1.4426950408889634f));
      wgt = __builtin_amdgcn_exp2f((mq - M) * 1.4426950408889634f) / Ls;
    }
    if (k == 0) STAMP(4);
    for (unsigned spin = 0; flg[4] < 4u && spin < (SPIN_CAP << 4); ++spin) __builtin_amdgcn_s_sleep(1);
    asm volatile("" ::: "memory");
    {
      const rsrc_t ro = mk(a.wo + (size_t)(h * 128 + s * 32) * 1024);
#pragma unroll
      for (int i = 0; i < 8; ++i) W[i] = ld16<AUX_NT>(ro, woff, i << 10);
    }
    unsigned P[4][8];
    pair_rows(W, P);
#pragma unroll
    for (int t = 0; t < 4; ++t)
#pragma unroll
      for (int j = 0; j < 8; ++j) asm volatile("" : "+v"(P[t][j]));
    if (k == 0) STAMP(5);
    {
      u32x4 g[1];
      gather<16, 1, 0, 0>(ws, WS_OP + (bh * 4 + s) * 1024 + gpos, tg, g);
      if (k == 0) STAMP(6);
      float lo = asf(g[0].x) * wgt, hi = asf(g[0].y) * wgt;
      lo += dpp<ROR4>(lo); hi += dpp<ROR4>(hi);
      lo += dpp<ROR8>(lo); hi += dpp<ROR8>(hi);
      const f32x2 r = gemv32(P, pk(lo, hi));
      put<16>(ws, WS_YP + (((b * 32 + 8 * k + (lane >> 3)) * 128 + h * 4 + s) * 8 + (lane & 7)) * 16, r.x, r.y, tg);
    }
    if (k == 0) STAMP(7);
    FLUSH();
  } else {
    if (k == 0 || (k == 1 && bid < 172)) {
      __builtin_amdgcn_s_setprio(3);
      const unsigned r8 = lane >> 3, c8 = lane & 7;
      const rsrc_t rx = mk(a.x);
      u32x4 X[2];
      X[0] = ld16<0>(rx, lane << 4, 0); X[1] = ld16<0>(rx, lane << 4, 1024);
      unsigned RS, g, par, dst; const char* tb;
      if (k == 0) {
        const unsigned jh = (x >> 1) & 1; g = x >> 2; par = (x ^ idx ^ (idx >> 4)) & 1;
        RS = 16384; tb = a.wq + (size_t)(g * 256 + par) * 8192 + (idx * 2 + jh) * 128;
        dst = WS_QP + (((idx * 2 + (lane >> 5)) * 4 + g * 2 + par) * 64 + (r8 & 3) * 16 + jh * 8 + c8) * 16;
      } else {
        const unsigned u = bid, mat = u >= 86, rem = u - mat * 86, j = rem >> 1; g = rem & 1;
        RS = 5504; tb = (mat ? a.wu : a.wg) + (size_t)g * (128 * 5504) + j * 128;
        const unsigned e32 = j * 2 + (c8 >> 2);
        par = e32 >= 43;
        const unsigned dd = e32 - par * 43;
        dst = WS_F1 + ((dd * 4 + mat * 2 + (lane >> 5)) * 64 + (par * 2 + g) * 16 + (c8 & 3) * 4 + (r8 & 3)) * 16;
      }
      u32x4 W[16];
      {
        const rsrc_t rt = mk(tb);
        const unsigned vo = r8 * 16 * RS + (c8 << 4);
#pragma unroll
        for (int i = 0; i < 16; ++i) W[i] = ld16<AUX_NT>(rt, vo, i * RS);
      }
      __builtin_amdgcn_sched_barrier(0);
      EPOCH_AND_BARRIER();
      {
        LAS unsigned char* XE = L + L_XE + k * 2048;
#pragma unroll
        for (int bb = 0; bb < 2; ++bb) {
          float e[8];
#pragma unroll
          for (int j = 0; j < 4; ++j) { e[2 * j] = blo(X[bb][j]); e[2 * j + 1] = bhi(X[bb][j]); }
          float sm = 0.f;
#pragma unroll
          for (int j = 0; j < 8; ++j) sm += e[j];
          const float mean = wave_sum(sm) * (1.0f / 512.0f);
          float sq = 0.f;
#pragma unroll
          for (int j = 0; j < 8; ++j) { const float dv = e[j] - mean; sq = fmaf(dv, dv, sq); }
          const float var = wave_sum(sq) * (1.0f / 512.0f);
          const float mb = rbf(mean);
          const float inv = 1.0f / rbf(__builtin_amdgcn_sqrtf(rbf(rbf(var) + 1e-5f)));
#pragma unroll
          for (int j = 0; j < 8; ++j) e[j] = rbf(e[j] - mb) * inv;
          u32x2 ev = {pk(e[0], e[2]), pk(e[4], e[6])}, od = {pk(e[1], e[3]), pk(e[5], e[7])};
          *(LAS u32x2*)(XE + bb * 1024 + lane * 8) = ev;
          *(LAS u32x2*)(XE + bb * 1024 + 512 + lane * 8) = od;
        }
        asm volatile("s_waitcnt lgkmcnt(0)" ::: "memory");
        u32x4 xa[2][2];
        const unsigned xo = par * 512 + g * 256 + r8 * 32;
#pragma unroll
        for (int bb = 0; bb < 2; ++bb) { xa[bb][0] = *(LAS u32x4*)(XE + bb * 1024 + xo); xa[bb][1] = *(LAS u32x4*)(XE + bb * 1024 + xo + 16); }
        float acc[2][8];
#pragma unroll
        for (int j = 0; j < 8; ++j) { acc[0][j] = 0.f; acc[1][j] = 0.f; }
#pragma unroll
        for (int t = 0; t < 8; ++t)
#pragma unroll
          for (int j = 0; j < 4; ++j) {
            const unsigned lo = plo(W[2 * t][j], W[2 * t + 1][j]), hi = phi(W[2 * t][j], W[2 * t + 1][j]);
#pragma unroll
            for (int bb = 0; bb < 2; ++bb) {
              acc[bb][2 * j] = dot2(lo, xa[bb][t >> 2][t & 3], acc[bb][2 * j]);
              acc[bb][2 * j + 1] = dot2(hi, xa[bb][t >> 2][t & 3], acc[bb][2 * j + 1]);
            }
          }
        float u[8], w4[4];
#pragma unroll
        for (int j = 0; j < 8; ++j) u[j] = tr32(acc[0][j], acc[1][j]);
#pragma unroll
        for (int j = 0; j < 4; ++j) w4[j] = tr16(u[j], u[j + 4]);
        float z[2];
#pragma unroll
        for (int j = 0; j < 2; ++j) { const float t1 = w4[j] + dpp<ROR8>(w4[j]), t2 = w4[j + 2] + dpp<ROR8>(w4[j + 2]); z[j] = (lane & 8) ? t2 : t1; }
        if (k == 0) put<1>(ws, dst, z[0], z[1], tg); else put<2>(ws, dst, z[0], z[1], tg);
      }
      if (k == 0) STAMP(8);
      __builtin_amdgcn_s_setprio(0);
    } else {
      if (k == 2 && lane < 10) flg[lane] = lane < 4 ? 0xffffffffu : 0u;
      EPOCH_AND_BARRIER();
    }
    const rsrc_t rv = mk(a.vc + kvbase);
    u32x4 V[32];
#pragma unroll
    for (int g = 0; g < 8; ++g) {
      pace(t0, 160 + (g * 4 + (int)k) * 4);
#pragma unroll
      for (int i = 4 * g; i < 4 * g + 4; ++i) V[i] = ld16<AUX_NT>(rv, v0 ^ (i << 12), 0);
      __builtin_amdgcn_sched_barrier(0);
    }
    if (lane == 0) __hip_atomic_fetch_add((LAS unsigned*)(L + L_FLG + 16), 1u, __ATOMIC_RELAXED, __HIP_MEMORY_SCOPE_WORKGROUP);
    if (k == 3) STAMP(9);
    u32x4 mk4;
    for (unsigned spin = 0;; ++spin) {
      mk4 = *(volatile LAS u32x4*)(L + L_FLG);
      if ((mk4.x != 0xffffffffu && mk4.y != 0xffffffffu && mk4.z != 0xffffffffu && mk4.w != 0xffffffffu) || spin > (SPIN_CAP << 4)) break;
      __builtin_amdgcn_s_sleep(1);
    }
    asm volatile("" ::: "memory");
    u32x4 pp[4];
#pragma unroll
    for (int i = 0; i < 4; ++i) pp[i] = *(LAS u32x4*)(L + L_PB + k * 256 + rg * 64 + i * 16);
    const float mown = asf(k == 0 ? mk4.x : k == 1 ? mk4.y : k == 2 ? mk4.z : mk4.w);
    const float cf = __builtin_amdgcn_exp2f((mown - fmaxf(fmaxf(asf(mk4.x), asf(mk4.y)), fmaxf(asf(mk4.z), asf(mk4.w)))) * 1.4426950408889634f);
    float acc[8];
#pragma unroll
    for (int j = 0; j < 8; ++j) acc[j] = 0.f;
#pragma unroll
    for (int t = 0; t < 16; ++t) {
#pragma unroll
      for (int j = 0; j < 4; ++j) {
        acc[2 * j] = dot2(plo(V[2 * t][j], V[2 * t + 1][j]), pp[t >> 2][t & 3], acc[2 * j]);
        acc[2 * j + 1] = dot2(phi(V[2 * t][j], V[2 * t + 1][j]), pp[t >> 2][t & 3], acc[2 * j + 1]);
      }
      __builtin_amdgcn_sched_barrier(0);
    }
    asm volatile("" :: "v"(acc[0]), "v"(acc[1]), "v"(acc[2]), "v"(acc[3]), "v"(acc[4]), "v"(acc[5]), "v"(acc[6]), "v"(acc[7]));
    if (k == 3) STAMP(10);
    {
      float u[4];
#pragma unroll
      for (int j = 0; j < 4; ++j) u[j] = tr32(acc[j], acc[j + 4]);
      f32x2 r = {tr16(u[0], u[2]) * cf, tr16(u[1], u[3]) * cf};
      *(LAS f32x2*)(L + L_OB + k * 512 + (c * 4 + rg) * 8) = r;
      asm volatile("s_waitcnt lgkmcnt(0)" ::: "memory");
      unsigned old = 0;
      if (lane == 0) old = __hip_atomic_fetch_add((LAS unsigned*)(L + L_FLG + 32), 1u, __ATOMIC_RELAXED, __HIP_MEMORY_SCOPE_WORKGROUP);
      old = __builtin_amdgcn_readfirstlane(old);
      asm volatile("" ::: "memory");
      if (old == 3u) {
        f32x2 o4[4];
#pragma unroll
        for (int i = 0; i < 4; ++i) o4[i] = *(LAS f32x2*)(L + L_OB + i * 512 + lane * 8);
        const f32x2 o = (o4[0] + o4[1]) + (o4[2] + o4[3]);
        put<16>(ws, WS_OP + ((bh * 4 + rg) * 64 + s * 16 + c) * 16, o.x, o.y, tg);
        STAMP(11);
      }
    }
    if (s == 0) {
      const unsigned slot = lane & 7;
      const unsigned col = (h >> 3) * 128 + (h & 1) * 64 + slot * 8 + ((h >> 1) & 3) * 2;
      const unsigned xw = __builtin_amdgcn_raw_buffer_load_b32(mk(a.x), (b * 512 + col) * 2, 0, 0);
      float f0 = 0.f, f1 = 0.f;
      if (k < 3) {
        u32x4 g[2];
        const unsigned ln1 = k * 16 + 8 + (lane >> 3);
        const bool v1 = ln1 < 43;
        const unsigned base = WS_F2 + bh * (48 * 128) + k * 2048 + lane * 16;
        for (unsigned spin = 0;; ++spin) {
          asm volatile("" ::: "memory");
          g[0] = ld16<AUX_SC1>(ws, base, 0); g[1] = ld16<AUX_SC1>(ws, base, 1024);
          if (ABL || __all(good(g[0], tg) & (good(g[1], tg) | !v1)) || spin > SPIN_CAP) break;
        }
        f0 = asf(g[0].x) + (v1 ? asf(g[1].x) : 0.f); f1 = asf(g[0].y) + (v1 ? asf(g[1].y) : 0.f);
        f0 += dpp<ROR8>(f0); f1 += dpp<ROR8>(f1);
        f0 = tr16(f0, f0); f1 = tr16(f1, f1);
        f0 = tr32(f0, f0); f1 = tr32(f1, f1);
      }
      if (k == 0) STAMP(12);
      u32x4 g[4];
      gather<16, 4, 1024, 0>(ws, WS_YP + bh * 16384 + k * 4096 + lane * 16, tg, g);
      if (k == 0) STAMP(13);
      float y0 = (asf(g[0].x) + asf(g[1].x)) + (asf(g[2].x) + asf(g[3].x));
      float y1 = (asf(g[0].y) + asf(g[1].y)) + (asf(g[2].y) + asf(g[3].y));
      y0 += dpp<ROR8>(y0); y1 += dpp<ROR8>(y1);
      y0 = tr16(y0, y0); y1 = tr16(y1, y1);
      y0 = tr32(y0, y0); y1 = tr32(y1, y1);
      if (lane < 8) { f32x4 r = {y0, y1, f0, f1}; *(LAS f32x4*)(L + L_FB + k * 128 + lane * 16) = r; }
      asm volatile("s_waitcnt lgkmcnt(0)" ::: "memory");
      unsigned old = 0;
      if (lane == 0) old = __hip_atomic_fetch_add((LAS unsigned*)(L + L_FLG + 36), 1u, __ATOMIC_RELAXED, __HIP_MEMORY_SCOPE_WORKGROUP);
      old = __builtin_amdgcn_readfirstlane(old);
      asm volatile("" ::: "memory");
      if (old == 3u) {
        f32x4 r4[4];
#pragma unroll
        for (int i = 0; i < 4; ++i) r4[i] = *(LAS f32x4*)(L + L_FB + i * 128 + slot * 16);
        const f32x4 r = (r4[0] + r4[1]) + (r4[2] + r4[3]);
        const float g0 = rbf(silu(rbf(r.z))), g1 = rbf(silu(rbf(r.w)));
        const unsigned o = pk(blo(xw) + rbf(rbf(r.x) * g0), bhi(xw) + rbf(rbf(r.y) * g1));
        if (lane < 8) __builtin_amdgcn_raw_buffer_store_b32(o, mk(a.out), (b * 512 + col) * 2, 0, 0);
        if (bid == 0 && lane == 0) { u32x2 e2 = {(unsigned)ep, (unsigned)(ep >> 32)}; __builtin_amdgcn_raw_buffer_store_b64(e2, ws, WS_EPOCH, 0, AUX_SC1); }
        STAMP(14);
      }
    }
    FLUSH();
  }
}

extern "C" void kernel_launch(void* const* d_in, const int* in_sizes, int n_in,
                              void* d_out, int out_size, void* d_ws, size_t ws_size,
                              hipStream_t stream) {
  Args a;
  a.x = (const char*)d_in[0]; a.kc = (const char*)d_in[1]; a.vc = (const char*)d_in[2];
  a.wq = (const char*)d_in[3]; a.wo = (const char*)d_in[4];
  a.wg = (const char*)d_in[6]; a.wu = (const char*)d_in[7]; a.wd = (const char*)d_in[8];
  a.out = (char*)d_out; a.ws = (char*)d_ws;
  fused_decode<<<256, 512, 0, stream>>>(a);
}
```

```cpp
#include <hip/hip_runtime.h>
#include <hip/hip_bf16.h>
#include <stdint.h>

typedef unsigned u32x4 __attribute__((ext_vector_type(4)));
typedef unsigned u32x2 __attribute__((ext_vector_type(2)));
typedef float f32x2 __attribute__((ext_vector_type(2)));
typedef float f32x4 __attribute__((ext_vector_type(4)));
typedef __bf16 bf16x2 __attribute__((ext_vector_type(2)));
#define LAS __attribute__((address_space(3)))
typedef __amdgpu_buffer_rsrc_t rsrc_t;

#define PACE_Q8 940
#define PACE_BURST -20
#define STAMPS 0

#define CODE_PF 1
#define OP_SLEEP 0
#define YP_SLEEP 0
#define CODE_BACK 0
#define CODE_LEN 10400
#define PREW 1
#define YP_DELAY 0
#define PMASK 0
#define GMASK 0
#define ABL 0
#define PUT_AUX 16
#define GET_AUX 16
#define WS_SHIFT 0
constexpr int AUX_NT = 2, AUX_SC1 = 16;
constexpr unsigned SPIN_CAP = 1u << 15;

constexpr unsigned WS_EPOCH = 0;
constexpr unsigned WS_ML = 4096;
constexpr unsigned WS_OP = WS_ML + 64 * 16 * 16;
constexpr unsigned WS_QP = WS_OP + 256 * 64 * 16;
constexpr unsigned WS_F1 = WS_QP + 64 * 256 * 16;
constexpr unsigned WS_F2 = WS_F1 + 43 * 256 * 16;
constexpr unsigned WS_YP = WS_F2 + 64 * 48 * 8 * 16;
constexpr unsigned WS_END = WS_YP + 64 * 128 * 8 * 16;

constexpr int L_XE = 0;
constexpr int L_PB = 4096;
constexpr int L_OB = 5120;
constexpr int L_FB = 7168;
constexpr int L_FLG = 7680;
constexpr int L_QB = 7744;
constexpr int L_HB = 8000;
constexpr int L_END = 8128;

struct Args {
  const char* kc; const char* vc; const char* x; const char* wq;
  const char* wg; const char* wu; const char* wd; char* ws; const char* wo; char* out;
};

__device__ __forceinline__ rsrc_t mk(const void* p) { return __builtin_amdgcn_make_buffer_rsrc((void*)p, 0, 0x7fffffff, 0x00020000); }
template <int AUX> __device__ __forceinline__ u32x4 ld16(rsrc_t r, unsigned voff, unsigned soff) { return __builtin_amdgcn_raw_buffer_load_b128(r, voff, soff, AUX); }
__device__ __forceinline__ float asf(unsigned u) { return __builtin_bit_cast(float, u); }
__device__ __forceinline__ unsigned asu(float f) { return __builtin_bit_cast(unsigned, f); }
__device__ __forceinline__ float dot2(unsigned a, unsigned b, float c) {
  return __builtin_amdgcn_fdot2_f32_bf16(__builtin_bit_cast(bf16x2, a), __builtin_bit_cast(bf16x2, b), c, false);
}
__device__ __forceinline__ unsigned pk(float lo, float hi) { f32x2 v = {lo, hi}; return __builtin_bit_cast(unsigned, __builtin_convertvector(v, bf16x2)); }
__device__ __forceinline__ float rbf(float f) { return (float)(__bf16)f; }
__device__ __forceinline__ float blo(unsigned u) { return asf(u << 16); }
__device__ __forceinline__ float bhi(unsigned u) { return asf(u & 0xffff0000u); }
__device__ __forceinline__ unsigned plo(unsigned a, unsigned b) { return __builtin_amdgcn_perm(b, a, 0x05040100u); }
__device__ __forceinline__ unsigned phi(unsigned a, unsigned b) { return __builtin_amdgcn_perm(b, a, 0x07060302u); }
template <int CTRL> __device__ __forceinline__ float dpp(float x) { return asf(__builtin_amdgcn_mov_dpp(asu(x), CTRL, 0xf, 0xf, true)); }
template <int CTRL> __device__ __forceinline__ unsigned dppu(unsigned x) { return __builtin_amdgcn_mov_dpp(x, CTRL, 0xf, 0xf, true); }
constexpr int XOR1 = 0xB1, XOR2 = 0x4E, XOR7 = 0x141, ROR8 = 0x128, ROR4 = 0x124;
__device__ __forceinline__ float tr32(float a, float b) { auto s = __builtin_amdgcn_permlane32_swap(asu(a), asu(b), false, false); return asf(s[0]) + asf(s[1]); }
__device__ __forceinline__ float tr16(float a, float b) { auto s = __builtin_amdgcn_permlane16_swap(asu(a), asu(b), false, false); return asf(s[0]) + asf(s[1]); }
__device__ __forceinline__ float mx32(float a) { auto s = __builtin_amdgcn_permlane32_swap(asu(a), asu(a), false, false); return fmaxf(asf(s[0]), asf(s[1])); }
__device__ __forceinline__ float mx16(float a) { auto s = __builtin_amdgcn_permlane16_swap(asu(a), asu(a), false, false); return fmaxf(asf(s[0]), asf(s[1])); }
__device__ __forceinline__ float row_sum(float v) { v += dpp<XOR1>(v); v += dpp<XOR2>(v); v += dpp<XOR7>(v); v += dpp<ROR8>(v); return v; }
__device__ __forceinline__ float row_max(float v) { v = fmaxf(v, dpp<XOR1>(v)); v = fmaxf(v, dpp<XOR2>(v)); v = fmaxf(v, dpp<XOR7>(v)); v = fmaxf(v, dpp<ROR8>(v)); return v; }
__device__ __forceinline__ float wave_sum(float v) { v = row_sum(v); v = tr16(v, v); return tr32(v, v); }
__device__ __forceinline__ float wave_max(float v) { v = row_max(v); v = mx16(v); return mx32(v); }
__device__ __forceinline__ float silu(float g) { return g * __builtin_amdgcn_rcpf(1.0f + __builtin_amdgcn_exp2f(-1.4426950408889634f * g)); }

struct Tag { unsigned t0, t1; };
template <int HOP> __device__ __forceinline__ void put(rsrc_t ws, unsigned off, float a, float b, Tag tg) {
  u32x4 g = {asu(a), asu(b), asu(a) ^ tg.t0, asu(b) ^ tg.t1};
  if (PMASK & HOP) asm volatile("" :: "v"(g)); else __builtin_amdgcn_raw_buffer_store_b128(g, ws, off, 0, PUT_AUX);
}
__device__ __forceinline__ bool good(u32x4 g, Tag tg) { return (((g.x ^ g.z ^ tg.t0) | (g.y ^ g.w ^ tg.t1)) == 0u); }
template <int HOP, int N, int STRIDE, int SLEEP> __device__ __forceinline__ void gather(rsrc_t ws, unsigned off, Tag tg, u32x4 (&v)[N]) {
  for (unsigned spin = 0;; ++spin) {
    asm volatile("" ::: "memory");
#pragma unroll
    for (int i = 0; i < N; ++i) { if (GMASK & HOP) { v[i] = u32x4{off, off, off, off}; asm volatile("" : "+v"(v[i])); } else v[i] = ld16<GET_AUX>(ws, off, i * STRIDE); }
    bool ok = true;
#pragma unroll
    for (int i = 0; i < N; ++i) ok &= good(v[i], tg);
    if (ABL || __all(ok) || spin > SPIN_CAP) break;
    if (SLEEP) __builtin_amdgcn_s_sleep(SLEEP);
  }
}
__device__ __forceinline__ unsigned now32() { return (unsigned)__builtin_amdgcn_s_memrealtime(); }
__device__ __forceinline__ void pace(unsigned t0, int kb) {
  const int tgt = ((kb * PACE_Q8) >> 8) - PACE_BURST;
  while ((int)(now32() - t0) < tgt) __builtin_amdgcn_s_sleep(1);
  __builtin_amdgcn_sched_barrier(0);
}
__device__ __forceinline__ void wg_barrier() { asm volatile("s_waitcnt lgkmcnt(0)" ::: "memory"); __builtin_amdgcn_s_barrier(); asm volatile("" ::: "memory"); __builtin_amdgcn_sched_barrier(0); }

#define STAMP(i) do {} while (0)
#define FLUSH() do {} while (0)

__device__ __forceinline__ void pair_rows(const u32x4 (&W)[8], unsigned (&P)[4][8]) {
#pragma unroll
  for (int t = 0; t < 4; ++t)
#pragma unroll
    for (int j = 0; j < 4; ++j) { P[t][2 * j] = plo(W[2 * t][j], W[2 * t + 1][j]); P[t][2 * j + 1] = phi(W[2 * t][j], W[2 * t + 1][j]); }
}
__device__ __forceinline__ f32x2 gemv32(const unsigned (&P)[4][8], unsigned opk) {
  const unsigned o0 = dppu<0x00>(opk), o1 = dppu<0x55>(opk), o2 = dppu<0xAA>(opk), o3 = dppu<0xFF>(opk);
  float acc[8];
#pragma unroll
  for (int j = 0; j < 8; ++j) acc[j] = dot2(P[3][j], o3, dot2(P[2][j], o2, dot2(P[1][j], o1, dot2(P[0][j], o0, 0.f))));
  float u[4];
#pragma unroll
  for (int j = 0; j < 4; ++j) u[j] = tr32(acc[j], acc[j + 4]);
  f32x2 r = {tr16(u[0], u[2]), tr16(u[1], u[3])};
  return r;
}

__global__ void __launch_bounds__(512, 2) fused_decode(Args a) {
  __shared__ __attribute__((aligned(16))) unsigned char smem[L_END];
  LAS unsigned char* L = (LAS unsigned char*)smem;
  const unsigned t0 = now32();
  const unsigned bid = blockIdx.x, x = bid & 7, idx = bid >> 3;
  const unsigned lane = threadIdx.x & 63, wave = __builtin_amdgcn_readfirstlane(threadIdx.x >> 6);
  const unsigned h = x * 4 + (idx >> 3), b = (idx >> 2) & 1, s = idx & 3, bh = b * 32 + h;
  const unsigned k = wave & 3;
  const bool kw = wave < 4;
  const rsrc_t rkv = mk((kw ? a.kc : a.vc) + ((size_t)bh * 8192 + 6144 + s * 512) * 256);
  const unsigned v0 = ((k << 13) | (lane << 4)) ^ (((bid * 2654435761u) >> 12) & 0x1fc00u);
#define KVOFF(i) ((((i) & 7) << 10) | (((i) >> 3) << 15))
  u32x4 A[32];
#pragma unroll
  for (int i = 0; i < 4; ++i) A[i] = ld16<AUX_NT>(rkv, v0 ^ KVOFF(i), 0);
  __builtin_amdgcn_sched_barrier(0);
  if (wave == 0) STAMP(12);

  unsigned long long ep;
  Tag tg;
#define EPOCH_AND_BARRIER() do { const char* wsp_ = a.ws + WS_SHIFT; asm volatile("" : "+s"(wsp_)); \
    ep = *(const __attribute__((address_space(4))) unsigned long long*)(wsp_ + WS_EPOCH) + 1ull; \
    tg.t0 = (unsigned)ep; tg.t1 = (unsigned)(ep >> 32) | 0x80000000u; \
    asm volatile("" :: "s"(tg.t0), "s"(tg.t1), "s"(a.wo), "s"(a.out)); wg_barrier(); } while (0)
  const rsrc_t ws = mk(a.ws + WS_SHIFT);
  volatile LAS unsigned* flg = (volatile LAS unsigned*)(L + L_FLG);
  const unsigned rg = lane >> 4, c = lane & 15;
  const bool has_f2 = bid >= 213;
  const unsigned d = bid - 213;
  const unsigned woff = (rg << 13) | (k << 8) | (c << 4);
  u32x4 W8[8];

  if (!kw && ((k < 2 && bid < 213) || (k == 0 && bid >= 254))) {
      __builtin_amdgcn_s_setprio(3);
      const unsigned r8 = lane >> 3, c8 = lane & 7;
      const rsrc_t rx = mk(a.x);
      u32x4 X[2];
      X[0] = ld16<0>(rx, lane << 4, 0); X[1] = ld16<0>(rx, lane << 4, 1024);
      unsigned RS, g, par, dst; const char* tb;
      const bool isq = k == 0 || bid >= 172;
      if (isq) {
        const unsigned vb = k == 0 ? bid : bid + 41, vx = vb & 7, vi = vb >> 3;
        const unsigned jh = (vx >> 1) & 1; g = vx >> 2; par = (vx ^ vi ^ (vi >> 4)) & 1;
        RS = 16384; tb = a.wq + (size_t)(g * 256 + par) * 8192 + (vi * 2 + jh) * 128;
        dst = WS_QP + (((vi * 2 + (lane >> 5)) * 4 + g * 2 + par) * 64 + (r8 & 3) * 16 + jh * 8 + c8) * 16;
      } else {
        const unsigned u = bid, mat = u >= 86, rem = u - mat * 86, j = rem >> 1; g = rem & 1;
        RS = 5504; tb = (mat ? a.wu : a.wg) + (size_t)g * (128 * 5504) + j * 128;
        const unsigned e32 = j * 2 + (c8 >> 2);
        par = e32 >= 43;
        const unsigned dd = e32 - par * 43;
        dst = WS_F1 + ((dd * 4 + mat * 2 + (lane >> 5)) * 64 + (par * 2 + g) * 16 + (c8 & 3) * 4 + (r8 & 3)) * 16;
      }
      u32x4 W[16];
      {
        const rsrc_t rt = mk(tb);
        const unsigned vo = r8 * 16 * RS + (c8 << 4);
#pragma unroll
        for (int i = 0; i < 16; ++i) W[i] = ld16<AUX_NT>(rt, vo, i * RS);
      }
      __builtin_amdgcn_sched_barrier(0);
      EPOCH_AND_BARRIER();
      {
        LAS unsigned char* XE = L + L_XE + k * 2048;
#pragma unroll
        for (int bb = 0; bb < 2; ++bb) {
          float e[8];
#pragma unroll
          for (int j = 0; j < 4; ++j) { e[2 * j] = blo(X[bb][j]); e[2 * j + 1] = bhi(X[bb][j]); }
          float sm = 0.f;
#pragma unroll
          for (int j = 0; j < 8; ++j) sm += e[j];
          const float mean = wave_sum(sm) * (1.0f / 512.0f);
          float sq = 0.f;
#pragma unroll
          for (int j = 0; j < 8; ++j) { const float dv = e[j] - mean; sq = fmaf(dv, dv, sq); }
          const float var = wave_sum(sq) * (1.0f / 512.0f);
          const float mb = rbf(mean);
          const float inv = 1.0f / rbf(__builtin_amdgcn_sqrtf(rbf(rbf(var) + 1e-5f)));
#pragma unroll
          for (int j = 0; j < 8; ++j) e[j] = rbf(e[j] - mb) * inv;
          u32x2 ev = {pk(e[0], e[2]), pk(e[4], e[6])}, od = {pk(e[1], e[3]), pk(e[5], e[7])};
          *(LAS u32x2*)(XE + bb * 1024 + lane * 8) = ev;
          *(LAS u32x2*)(XE + bb * 1024 + 512 + lane * 8) = od;
        }
        asm volatile("s_waitcnt lgkmcnt(0)" ::: "memory");
        __builtin_amdgcn_sched_barrier(0);
        u32x4 xa[2][2];
        const unsigned xo = par * 512 + g * 256 + r8 * 32;
#pragma unroll
        for (int bb = 0; bb < 2; ++bb) { xa[bb][0] = *(LAS u32x4*)(XE + bb * 1024 + xo); xa[bb][1] = *(LAS u32x4*)(XE + bb * 1024 + xo + 16); }
        float acc[2][8];
#pragma unroll
        for (int j = 0; j < 8; ++j) { acc[0][j] = 0.f; acc[1][j] = 0.f; }
#pragma unroll
        for (int t = 0; t < 8; ++t)
#pragma unroll
          for (int j = 0; j < 4; ++j) {
            const unsigned lo = plo(W[2 * t][j], W[2 * t + 1][j]), hi = phi(W[2 * t][j], W[2 * t + 1][j]);
#pragma unroll
            for (int bb = 0; bb < 2; ++bb) {
              acc[bb][2 * j] = dot2(lo, xa[bb][t >> 2][t & 3], acc[bb][2 * j]);
              acc[bb][2 * j + 1] = dot2(hi, xa[bb][t >> 2][t & 3], acc[bb][2 * j + 1]);
            }
          }
        float u[8], w4[4];
#pragma unroll
        for (int j = 0; j < 8; ++j) u[j] = tr32(acc[0][j], acc[1][j]);
#pragma unroll
        for (int j = 0; j < 4; ++j) w4[j] = tr16(u[j], u[j + 4]);
        float z[2];
#pragma unroll
        for (int j = 0; j < 2; ++j) { const float t1 = w4[j] + dpp<ROR8>(w4[j]), t2 = w4[j + 2] + dpp<ROR8>(w4[j + 2]); z[j] = (lane & 8) ? t2 : t1; }
        put<3>(ws, dst, z[0], z[1], tg);
      }
      if (k == 0) STAMP(8);
      __builtin_amdgcn_s_setprio(0);
  } else {
    if (kw && has_f2) {
      const rsrc_t rd = mk(a.wd + (size_t)d * 32768);
#pragma unroll
      for (int i = 0; i < 8; ++i) W8[i] = ld16<AUX_NT>(rd, woff, i << 10);
    }
    if (wave == 6) { if (lane < 16) flg[lane] = lane < 4 ? 0xffffffffu : 0u; *(LAS u32x2*)(L + L_FB + lane * 8) = u32x2{0u, 0u}; }
    EPOCH_AND_BARRIER();
    if (wave == 7) __builtin_amdgcn_raw_buffer_store_b128(u32x4{0u, 0u, 0u, 0u}, ws, WS_OP + ((bh * 4 + (c >> 2)) * 64 + s * 16 + rg * 4 + (c & 3)) * 16, 0, AUX_SC1);
    if (wave == 7) {
      const char* pc = (const char*)((__builtin_amdgcn_s_getpc() - CODE_BACK) & ~127ull);
      const unsigned v = __builtin_amdgcn_raw_buffer_load_b32(__builtin_amdgcn_make_buffer_rsrc((void*)pc, 0, CODE_LEN, 0x00020000), ((idx & 15) * 8 + (lane & 7)) * 128, 0, 0);
      asm volatile("" :: "v"(v));
    }
  }
  {
    const int pos0 = (kw ? 48 : 160) + 4 * (int)k;
#pragma unroll
    for (int g = 1; g < 8; ++g) {
      pace(t0, pos0 + g * 16);
#pragma unroll
      for (int i = 4 * g; i < 4 * g + 4; ++i) A[i] = ld16<AUX_NT>(rkv, v0 ^ KVOFF(i), 0);
      __builtin_amdgcn_sched_barrier(0);
    }
  }

  if (kw) {
    if (k == 0) STAMP(1);
    const unsigned gpos = (((c >> 2) * 16) | (rg << 2) | (c & 3)) * 16;
    if (k == 0) {
      u32x4 g[4], qq;
      gather<1, 4, 256, 16>(ws, WS_QP + (((h * 2 + b) * 4 + rg) * 64 + c) * 16, tg, g);
#pragma unroll
      for (int i = 0; i < 4; ++i) {
        float lo = asf(g[i].x), hi = asf(g[i].y);
        lo = tr16(lo, lo); hi = tr16(hi, hi);
        lo = tr32(lo, lo); hi = tr32(hi, hi);
        qq[i] = pk(lo, hi);
      }
      if (lane < 16) *(LAS u32x4*)(L + L_QB + lane * 16) = qq;
      asm volatile("s_waitcnt lgkmcnt(0)" ::: "memory");
      if (lane == 0) flg[5] = 1u;
    } else if (k == 1 && has_f2) {
      u32x4 g[4];
      gather<2, 4, 1024, 16>(ws, WS_F1 + d * 4096 + gpos, tg, g);
      float v[4][2];
#pragma unroll
      for (int i = 0; i < 4; ++i) {
        float lo = asf(g[i].x), hi = asf(g[i].y);
        lo += dpp<ROR4>(lo); hi += dpp<ROR4>(hi);
        lo += dpp<ROR8>(lo); hi += dpp<ROR8>(hi);
        v[i][0] = rbf(lo); v[i][1] = rbf(hi);
      }
#pragma unroll
      for (int bb = 0; bb < 2; ++bb) {
        const unsigned hp = pk(rbf(silu(v[bb][0])) * v[2 + bb][0], rbf(silu(v[bb][1])) * v[2 + bb][1]);
        if (c < 4) ((LAS unsigned*)(L + L_HB))[bb * 16 + rg * 4 + c] = hp;
      }
      asm volatile("s_waitcnt lgkmcnt(0)" ::: "memory");
      if (lane == 0) flg[6] = 1u;
    }
    for (unsigned spin = 0; flg[5] == 0u && spin < (SPIN_CAP << 4); ++spin) __builtin_amdgcn_s_sleep(1);
    asm volatile("" ::: "memory");
    const u32x4 q = *(LAS u32x4*)(L + L_QB + c * 16);
    if (k == 0) STAMP(2);
    float xs[32];
#pragma unroll
    for (int i = 0; i < 32; ++i) xs[i] = dot2(A[i].w, q[3], dot2(A[i].z, q[2], dot2(A[i].y, q[1], dot2(A[i].x, q[0], 0.f))));
#define TR_STEP(HALF, CTRL, BIT) _Pragma("unroll") for (int i = 0; i < (HALF); ++i) { \
      const float t1_ = xs[i] + dpp<CTRL>(xs[i]), t2_ = xs[i + (HALF)] + dpp<CTRL>(xs[i + (HALF)]); xs[i] = (c & (BIT)) ? t2_ : t1_; }
    TR_STEP(16, ROR8, 8) TR_STEP(8, XOR7, 4) TR_STEP(4, XOR2, 2) TR_STEP(2, XOR1, 1)
#undef TR_STEP
    const float s0 = rbf(rbf(xs[0]) * 0.08838834764831845f), s1 = rbf(rbf(xs[1]) * 0.08838834764831845f);
    const float m = wave_max(fmaxf(s0, s1));
    const float p0 = __builtin_amdgcn_exp2f((s0 - m) * 1.4426950408889634f), p1 = __builtin_amdgcn_exp2f((s1 - m) * 1.4426950408889634f);
    ((LAS unsigned*)(L + L_PB))[k * 64 + lane] = pk(p0, p1);
    asm volatile("s_waitcnt lgkmcnt(0)" ::: "memory");
    if (lane == 0) flg[k] = asu(m);
    const float l = wave_sum(p0 + p1);
    if (lane == 0) put<8>(ws, WS_ML + (bh * 16 + s * 4 + k) * 16, m, l, tg);
    __builtin_amdgcn_raw_buffer_store_b128(u32x4{0u, 0u, 0u, 0u}, ws, WS_YP + (((b * 32 + 8 * k + (lane >> 3)) * 128 + h * 4 + s) * 8 + (lane & 7)) * 16, 0, AUX_SC1);
    if (k == 0) STAMP(3);
    if (has_f2) {
      unsigned P[4][8];
      pair_rows(W8, P);
      for (unsigned spin = 0; flg[6] == 0u && spin < (SPIN_CAP << 4); ++spin) __builtin_amdgcn_s_sleep(1);
      asm volatile("" ::: "memory");
#pragma unroll
      for (int bb = 0; bb < 2; ++bb) {
        const unsigned hp = ((LAS unsigned*)(L + L_HB))[bb * 16 + rg * 4 + (c & 3)];
        const f32x2 r = gemv32(P, hp);
        put<4>(ws, WS_F2 + (((bb * 32 + 8 * k + (lane >> 3)) * 48 + d) * 8 + (lane & 7)) * 16, r.x, r.y, tg);
      }
    }
    for (unsigned spin = 0; flg[4] < 4u && spin < (SPIN_CAP << 4); ++spin) __builtin_amdgcn_s_sleep(1);
    asm volatile("" ::: "memory");
    {
      const rsrc_t ro = mk(a.wo + (size_t)(h * 128 + s * 32) * 1024);
#pragma unroll
      for (int i = 0; i < 8; ++i) W8[i] = ld16<AUX_NT>(ro, woff, i << 10);
    }
    float wgt;
    {
      u32x4 g[1];
      gather<8, 1, 0, 0>(ws, WS_ML + (bh * 16 + c) * 16, tg, g);
      const float mi = asf(g[0].x), li = asf(g[0].y);
      float mq = fmaxf(mi, dpp<XOR1>(mi)); mq = fmaxf(mq, dpp<XOR2>(mq));
      float M = fmaxf(mq, dpp<XOR7>(mq)); M = fmaxf(M, dpp<ROR8>(M));
      const float Ls = row_sum(li * __builtin_amdgcn_exp2f((mi - M) * 1.4426950408889634f));
      wgt = __builtin_amdgcn_exp2f((mq - M) * 1.4426950408889634f) * __builtin_amdgcn_rcpf(Ls);
    }
    unsigned P[4][8];
    pair_rows(W8, P);
#pragma unroll
    for (int t = 0; t < 4; ++t)
#pragma unroll
      for (int j = 0; j < 8; ++j) asm volatile("" : "+v"(P[t][j]));
    if (k == 0) STAMP(5);
    {
      u32x4 g[1];
      gather<16, 1, 0, OP_SLEEP>(ws, WS_OP + (bh * 4 + s) * 1024 + (((c >> 2) * 16) | ((c & 3) << 2) | rg) * 16, tg, g);
      if (k == 0) STAMP(6);
      float lo = asf(g[0].x) * wgt, hi = asf(g[0].y) * wgt;
      lo += dpp<ROR4>(lo); hi += dpp<ROR4>(hi);
      lo += dpp<ROR8>(lo); hi += dpp<ROR8>(hi);
      const f32x2 r = gemv32(P, pk(lo, hi));
      put<16>(ws, WS_YP + (((b * 32 + 8 * k + (lane >> 3)) * 128 + h * 4 + s) * 8 + (lane & 7)) * 16, r.x, r.y, tg);
      if (lane == 0) flg[7] = 1u;
    }
    if (k == 0) STAMP(7);
    FLUSH();
  } else {
    if (lane == 0) __hip_atomic_fetch_add((LAS unsigned*)(L + L_FLG + 16), 1u, __ATOMIC_RELAXED, __HIP_MEMORY_SCOPE_WORKGROUP);
    if (k == 3) STAMP(9);
    u32x4 mk4;
    for (unsigned spin = 0;; ++spin) {
      mk4 = *(volatile LAS u32x4*)(L + L_FLG);
      if ((mk4.x != 0xffffffffu && mk4.y != 0xffffffffu && mk4.z != 0xffffffffu && mk4.w != 0xffffffffu) || spin > (SPIN_CAP << 4)) break;
      __builtin_amdgcn_s_sleep(1);
    }
    asm volatile("" ::: "memory");
    u32x4 pp[4];
#pragma unroll
    for (int i = 0; i < 4; ++i) pp[i] = *(LAS u32x4*)(L + L_PB + k * 256 + rg * 64 + i * 16);
    const float mown = asf(k == 0 ? mk4.x : k == 1 ? mk4.y : k == 2 ? mk4.z : mk4.w);
    const float cf = __builtin_amdgcn_exp2f((mown - fmaxf(fmaxf(asf(mk4.x), asf(mk4.y)), fmaxf(asf(mk4.z), asf(mk4.w)))) * 1.4426950408889634f);
    float acc[8];
#pragma unroll
    for (int j = 0; j < 8; ++j) acc[j] = 0.f;
    f32x2 oth = {0.f, 0.f};
#pragma unroll
    for (int t = 0; t < 16; ++t) {
      if (t == 15 && k == 3) {
        for (unsigned spin = 0; flg[8] < 3u && spin < (SPIN_CAP << 4); ++spin) {}
        asm volatile("" ::: "memory");
        oth = (*(LAS f32x2*)(L + L_OB + lane * 8) + *(LAS f32x2*)(L + L_OB + 512 + lane * 8)) + *(LAS f32x2*)(L + L_OB + 1024 + lane * 8);
        asm volatile("" : "+v"(oth));
      }
#pragma unroll
      for (int j = 0; j < 4; ++j) {
        acc[2 * j] = dot2(plo(A[2 * t][j], A[2 * t + 1][j]), pp[t >> 2][t & 3], acc[2 * j]);
        acc[2 * j + 1] = dot2(phi(A[2 * t][j], A[2 * t + 1][j]), pp[t >> 2][t & 3], acc[2 * j + 1]);
      }
      asm volatile("" : "+v"(acc[0]), "+v"(acc[1]), "+v"(acc[2]), "+v"(acc[3]), "+v"(acc[4]), "+v"(acc[5]), "+v"(acc[6]), "+v"(acc[7]));
      __builtin_amdgcn_sched_barrier(0);
    }
    if (k == 3) STAMP(10);
    {
      float u[4];
#pragma unroll
      for (int j = 0; j < 4; ++j) u[j] = tr32(acc[j], acc[j + 4]);
      f32x2 r = {fmaf(tr16(u[0], u[2]), cf, oth.x), fmaf(tr16(u[1], u[3]), cf, oth.y)};
      if (k == 3) {
        put<16>(ws, WS_OP + ((bh * 4 + (c >> 2)) * 64 + s * 16 + rg * 4 + (c & 3)) * 16, r.x, r.y, tg);
        STAMP(11);
      } else {
        *(LAS f32x2*)(L + L_OB + k * 512 + lane * 8) = r;
        asm volatile("s_waitcnt lgkmcnt(0)" ::: "memory");
        if (lane == 0) __hip_atomic_fetch_add((LAS unsigned*)(L + L_FLG + 32), 1u, __ATOMIC_RELAXED, __HIP_MEMORY_SCOPE_WORKGROUP);
      }
    }
    if (s == 0) {
      const unsigned slot = lane & 7;
      const unsigned col = (h >> 3) * 128 + (h & 1) * 64 + slot * 8 + ((h >> 1) & 3) * 2;
      float g0 = 0.f, g1 = 0.f; unsigned xw = 0;
      if (k == 0) {
        xw = __builtin_amdgcn_raw_buffer_load_b32(mk(a.x), (b * 512 + col) * 2, 0, 0);
        u32x4 g[6];
        const bool v5 = lane < 24;
        const unsigned base = WS_F2 + bh * (48 * 128) + lane * 16;
        for (unsigned spin = 0;; ++spin) {
          asm volatile("" ::: "memory");
#pragma unroll
          for (int i = 0; i < 6; ++i) g[i] = ld16<AUX_SC1>(ws, base, i * 1024);
          if (ABL || __all(good(g[0], tg) & good(g[1], tg) & good(g[2], tg) & good(g[3], tg) & good(g[4], tg) & (good(g[5], tg) | !v5)) || spin > SPIN_CAP) break;
          __builtin_amdgcn_s_sleep(4);
        }
        float f0 = ((asf(g[0].x) + asf(g[1].x)) + (asf(g[2].x) + asf(g[3].x))) + (asf(g[4].x) + (v5 ? asf(g[5].x) : 0.f));
        float f1 = ((asf(g[0].y) + asf(g[1].y)) + (asf(g[2].y) + asf(g[3].y))) + (asf(g[4].y) + (v5 ? asf(g[5].y) : 0.f));
        f0 += dpp<ROR8>(f0); f1 += dpp<ROR8>(f1);
        f0 = tr16(f0, f0); f1 = tr16(f1, f1);
        f0 = tr32(f0, f0); f1 = tr32(f1, f1);
        g0 = rbf(silu(rbf(f0))); g1 = rbf(silu(rbf(f1)));
        asm volatile("" : "+v"(g0), "+v"(g1));
      }
      for (unsigned spin = 0; flg[7] == 0u && spin < (SPIN_CAP << 4); ++spin) __builtin_amdgcn_s_sleep(1);
      if (YP_DELAY) __builtin_amdgcn_s_sleep(YP_DELAY);
      u32x4 g[4];
      gather<16, 4, 1024, YP_SLEEP>(ws, WS_YP + bh * 16384 + k * 4096 + lane * 16, tg, g);
      if (k == 0) STAMP(13);
      float y0 = (asf(g[0].x) + asf(g[1].x)) + (asf(g[2].x) + asf(g[3].x));
      float y1 = (asf(g[0].y) + asf(g[1].y)) + (asf(g[2].y) + asf(g[3].y));
      y0 += dpp<ROR8>(y0); y1 += dpp<ROR8>(y1);
      y0 = tr16(y0, y0); y1 = tr16(y1, y1);
      y0 = tr32(y0, y0); y1 = tr32(y1, y1);
      if (k != 0) {
        if (lane < 8) { u32x4 r = {asu(y0), asu(y1), 1u, 0u}; *(volatile LAS u32x4*)(L + L_FB + k * 128 + lane * 16) = r; }
      } else {
        u32x4 r1, r2, r3;
        for (unsigned spin = 0;; ++spin) {
          r1 = *(volatile LAS u32x4*)(L + L_FB + 128 + slot * 16); r2 = *(volatile LAS u32x4*)(L + L_FB + 256 + slot * 16); r3 = *(volatile LAS u32x4*)(L + L_FB + 384 + slot * 16);
          if (__all((r1.z & r2.z & r3.z) == 1u) || spin > (SPIN_CAP << 6)) break;
        }
        y0 = (y0 + asf(r1.x)) + (asf(r2.x) + asf(r3.x)); y1 = (y1 + asf(r1.y)) + (asf(r2.y) + asf(r3.y));
        const unsigned o = pk(blo(xw) + rbf(rbf(y0) * g0), bhi(xw) + rbf(rbf(y1) * g1));
        if (lane < 8) __builtin_amdgcn_raw_buffer_store_b32(o, mk(a.out), (b * 512 + col) * 2, 0, 0);
        if (bid == 0 && lane == 0) { u32x2 e2 = {(unsigned)ep, (unsigned)(ep >> 32)}; __builtin_amdgcn_raw_buffer_store_b64(e2, ws, WS_EPOCH, 0, AUX_SC1); }
        STAMP(14);
      }
    }
    FLUSH();
  }
}

extern "C" void kernel_launch(void* const* d_in, const int* in_sizes, int n_in,
                              void* d_out, int out_size, void* d_ws, size_t ws_size,
                              hipStream_t stream) {
  Args a;
  a.x = (const char*)d_in[0]; a.kc = (const char*)d_in[1]; a.vc = (const char*)d_in[2];
  a.wq = (const char*)d_in[3]; a.wo = (const char*)d_in[4];
  a.wg = (const char*)d_in[6]; a.wu = (const char*)d_in[7]; a.wd = (const char*)d_in[8];
  a.out = (char*)d_out; a.ws = (char*)d_ws;
  fused_decode<<<256, 512, 0, stream>>>(a);
}
```

```cpp
#include <hip/hip_runtime.h>
#include <hip/hip_bf16.h>
#include <stdint.h>

typedef unsigned u32x4 __attribute__((ext_vector_type(4)));
typedef unsigned u32x2 __attribute__((ext_vector_type(2)));
typedef float f32x2 __attribute__((ext_vector_type(2)));
typedef float f32x4 __attribute__((ext_vector_type(4)));
typedef __bf16 bf16x2 __attribute__((ext_vector_type(2)));
typedef __bf16 bf16x8 __attribute__((ext_vector_type(8)));
#define LAS __attribute__((address_space(3)))
typedef __amdgpu_buffer_rsrc_t rsrc_t;

#define PACE_Q8 920
#define PACE_BURST -30
#define STAMPS 0
#define CODE_PF 1
#define CODE_LEN 13312
#define PRET 1
#define WO_GATE 4u
#define WO_STEP 0
#define EP_AUX 0
#define OUT_AUX 0
#define A_PACE 4
#define WO_DELAY 5
#define YP_DELAY 0

constexpr int AUX_NT = 2, AUX_SC1 = 16;
constexpr unsigned SPIN_CAP = 1u << 15;
constexpr float LOG2E = 1.4426950408889634f;

constexpr unsigned WS_EPOCH = 0;
constexpr unsigned WS_ML = 4096;
constexpr unsigned WS_OP = WS_ML + 64 * 16 * 16;
constexpr unsigned WS_QP = WS_OP + 256 * 64 * 16;
constexpr unsigned WS_F1 = WS_QP + 64 * 128 * 16;
constexpr unsigned WS_F2 = WS_F1 + 43 * 128 * 16;
constexpr unsigned WS_YP = WS_F2 + 32 * 48 * 128;
constexpr unsigned WS_END = WS_YP + 32 * 128 * 128;

constexpr int L_XE = 0;
constexpr int L_PB = 4096;
constexpr int L_OB = 5120;
constexpr int L_FB = 7168;
constexpr int L_FLG = 9216;
constexpr int L_QB = 9280;
constexpr int L_HB = 9536;
constexpr int L_ZB = 9664;
constexpr int L_END = 10176;

struct Args {
  const char* kc; const char* vc; const char* x; const char* wq;
  const char* wg; const char* wu; const char* wd; char* ws; const char* wo; char* out;
};

__device__ __forceinline__ rsrc_t mk(const void* p) { return __builtin_amdgcn_make_buffer_rsrc((void*)p, 0, 0x7fffffff, 0x00020000); }
template <int AUX> __device__ __forceinline__ u32x4 ld16(rsrc_t r, unsigned voff, unsigned soff) { return __builtin_amdgcn_raw_buffer_load_b128(r, voff, soff, AUX); }
__device__ __forceinline__ float asf(unsigned u) { return __builtin_bit_cast(float, u); }
__device__ __forceinline__ unsigned asu(float f) { return __builtin_bit_cast(unsigned, f); }
__device__ __forceinline__ float dot2(unsigned a, unsigned b, float c) {
  return __builtin_amdgcn_fdot2_f32_bf16(__builtin_bit_cast(bf16x2, a), __builtin_bit_cast(bf16x2, b), c, false);
}
__device__ __forceinline__ f32x4 mfma(u32x4 a, u32x4 b, f32x4 c) {
  return __builtin_amdgcn_mfma_f32_16x16x32_bf16(__builtin_bit_cast(bf16x8, a), __builtin_bit_cast(bf16x8, b), c, 0, 0, 0);
}
__device__ __forceinline__ unsigned pk(float lo, float hi) { f32x2 v = {lo, hi}; return __builtin_bit_cast(unsigned, __builtin_convertvector(v, bf16x2)); }
__device__ __forceinline__ float rbf(float f) { return (float)(__bf16)f; }
__device__ __forceinline__ float blo(unsigned u) { return asf(u << 16); }
__device__ __forceinline__ float bhi(unsigned u) { return asf(u & 0xffff0000u); }
__device__ __forceinline__ unsigned plo(unsigned a, unsigned b) { return __builtin_amdgcn_perm(b, a, 0x05040100u); }
__device__ __forceinline__ unsigned phi(unsigned a, unsigned b) { return __builtin_amdgcn_perm(b, a, 0x07060302u); }
template <int CTRL> __device__ __forceinline__ float dpp(float x) { return asf(__builtin_amdgcn_mov_dpp(asu(x), CTRL, 0xf, 0xf, true)); }
template <int CTRL> __device__ __forceinline__ unsigned dppu(unsigned x) { return __builtin_amdgcn_mov_dpp(x, CTRL, 0xf, 0xf, true); }
constexpr int XOR1 = 0xB1, XOR2 = 0x4E, XOR7 = 0x141, ROR8 = 0x128, ROR4 = 0x124;
__device__ __forceinline__ float tr32(float a, float b) { auto s = __builtin_amdgcn_permlane32_swap(asu(a), asu(b), false, false); return asf(s[0]) + asf(s[1]); }
__device__ __forceinline__ float tr16(float a, float b) { auto s = __builtin_amdgcn_permlane16_swap(asu(a), asu(b), false, false); return asf(s[0]) + asf(s[1]); }
__device__ __forceinline__ float mx32(float a) { auto s = __builtin_amdgcn_permlane32_swap(asu(a), asu(a), false, false); return fmaxf(asf(s[0]), asf(s[1])); }
__device__ __forceinline__ float mx16(float a) { auto s = __builtin_amdgcn_permlane16_swap(asu(a), asu(a), false, false); return fmaxf(asf(s[0]), asf(s[1])); }
__device__ __forceinline__ float row_sum(float v) { v += dpp<XOR1>(v); v += dpp<XOR2>(v); v += dpp<XOR7>(v); v += dpp<ROR8>(v); return v; }
__device__ __forceinline__ float row_max(float v) { v = fmaxf(v, dpp<XOR1>(v)); v = fmaxf(v, dpp<XOR2>(v)); v = fmaxf(v, dpp<XOR7>(v)); v = fmaxf(v, dpp<ROR8>(v)); return v; }
__device__ __forceinline__ float wave_sum(float v) { v = row_sum(v); v = tr16(v, v); return tr32(v, v); }
__device__ __forceinline__ float wave_max(float v) { v = row_max(v); v = mx16(v); return mx32(v); }
__device__ __forceinline__ float silu(float g) { return g * __builtin_amdgcn_rcpf(1.0f + __builtin_amdgcn_exp2f(-LOG2E * g)); }

struct Tag { unsigned t0, t1; };
__device__ __forceinline__ void put2(rsrc_t ws, unsigned off, float a, float b, Tag tg) {
  u32x4 g = {asu(a), asu(b), asu(a) ^ tg.t0, asu(b) ^ tg.t1};
  __builtin_amdgcn_raw_buffer_store_b128(g, ws, off, 0, AUX_SC1);
}
__device__ __forceinline__ unsigned bad2(u32x4 g, Tag tg) { return (g.x ^ g.z ^ tg.t0) | (g.y ^ g.w ^ tg.t1); }
#define SLICE(j) ((tg.t0 + (j) * 0x555u) & 0xfffu)
__device__ __forceinline__ void put4(rsrc_t ws, unsigned off, float a, float b, float c, float d, u32x4 sl) {
  u32x4 g = {((asu(a) + 0x800u) & 0xfffff000u) | sl.x, ((asu(b) + 0x800u) & 0xfffff000u) | sl.y,
             ((asu(c) + 0x800u) & 0xfffff000u) | sl.z, ((asu(d) + 0x800u) & 0xfffff000u) | sl.w};
  __builtin_amdgcn_raw_buffer_store_b128(g, ws, off, 0, AUX_SC1);
}
__device__ __forceinline__ unsigned strip4(u32x4& g, Tag tg) {
  g.x ^= SLICE(0); g.y ^= SLICE(1); g.z ^= SLICE(2); g.w ^= SLICE(3);
  return g.x | g.y | g.z | g.w;
}
__device__ __forceinline__ bool none(unsigned bad) { asm volatile("" : "+v"(bad)); return __builtin_amdgcn_ballot_w64(bad != 0u) == 0ull; }
__device__ __forceinline__ bool nolane(bool cnd) { return __builtin_amdgcn_ballot_w64(cnd) == 0ull; }
template <int N, int STRIDE, int SLEEP> __device__ __forceinline__ void gather2(rsrc_t ws, unsigned off, Tag tg, u32x4 (&v)[N]) {
  unsigned spin = 0, bad;
#pragma nounroll
  do {
    asm volatile("" ::: "memory");
    if (SLEEP && spin) __builtin_amdgcn_s_sleep(SLEEP);
#pragma unroll
    for (int i = 0; i < N; ++i) v[i] = ld16<AUX_SC1>(ws, off, i * STRIDE);
    unsigned live = (unsigned)((int)(++spin - SPIN_CAP) >> 31);
    asm volatile("" : "+s"(live));
    bad = 0;
#pragma unroll
    for (int i = 0; i < N; ++i) bad |= bad2(v[i], tg);
    bad &= live;
  } while (!none(bad));
}
__device__ __forceinline__ unsigned now32() { return (unsigned)__builtin_amdgcn_s_memrealtime(); }
__device__ __forceinline__ void pace(unsigned t0, int kb) {
  const int tgt = ((kb * PACE_Q8) >> 8) - (PACE_BURST);
  while ((int)(now32() - t0) < tgt) __builtin_amdgcn_s_sleep(1);
  __builtin_amdgcn_sched_barrier(0);
}
__device__ __forceinline__ void wg_barrier() {
  asm volatile("s_waitcnt lgkmcnt(0)" ::: "memory"); __builtin_amdgcn_s_barrier(); asm volatile("" ::: "memory"); __builtin_amdgcn_sched_barrier(0);
}
#define LDS_WAIT(cond) do { for (unsigned spin_ = 0; !(cond) && spin_ < (SPIN_CAP << 4); ++spin_) __builtin_amdgcn_s_sleep(1); asm volatile("" ::: "memory"); } while (0)

#define STAMP(i) do {} while (0)
#define FLUSH() do {} while (0)

__device__ __forceinline__ void to_frags(const u32x4* W, u32x4 (&B)[8]) {
#pragma unroll
  for (int t = 0; t < 4; ++t)
#pragma unroll
    for (int j = 0; j < 4; ++j) { B[2 * j][t] = plo(W[2 * t][j], W[2 * t + 1][j]); B[2 * j + 1][t] = phi(W[2 * t][j], W[2 * t + 1][j]); }
}
__device__ __forceinline__ void gemv32_put(const u32x4 (&B)[8], u32x4 av, rsrc_t ws, unsigned off, bool up, u32x4 sl) {
  float d[8];
#pragma unroll
  for (int j = 0; j < 4; ++j) { d[j] = mfma(av, B[j], f32x4{0.f, 0.f, 0.f, 0.f})[0]; d[j + 4] = mfma(av, B[j + 4], f32x4{0.f, 0.f, 0.f, 0.f})[0]; }
  __builtin_amdgcn_sched_barrier(0);
  put4(ws, off, up ? d[4] : d[0], up ? d[5] : d[1], up ? d[6] : d[2], up ? d[7] : d[3], sl);
}

__global__ void __launch_bounds__(512, 2) fused_decode(const char* kc_, const char* vc_, const char* x_, const char* wq_, const char* wg_, const char* wu_, const char* wd_, char* ws_, const char* wo_, char* out_) {
  const Args a = {kc_, vc_, x_, wq_, wg_, wu_, wd_, ws_, wo_, out_};
  __shared__ __attribute__((aligned(16))) unsigned char smem[L_END];
  LAS unsigned char* L = (LAS unsigned char*)smem;
  const unsigned t0 = now32();
  const unsigned bid = blockIdx.x, x = bid & 7, idx = bid >> 3;
  const unsigned lane = threadIdx.x & 63, wave = __builtin_amdgcn_readfirstlane(threadIdx.x >> 6);
  const unsigned h = x * 4 + (idx >> 3), b = (idx >> 2) & 1, s = idx & 3, bh = b * 32 + h;
  const unsigned k = wave & 3;
  const bool kw = wave < 4;
  const rsrc_t rkv = mk((kw ? a.kc : a.vc) + ((size_t)bh * 8192 + 6144 + s * 512) * 256);
  const unsigned v0 = ((k << 13) | (lane << 4)) ^ (((bid * 2654435761u) >> 12) & 0x1fc00u);
#define KVOFF(i) ((((i) & 7) << 10) | (((i) >> 3) << 15))
  u32x4 A[32];
#pragma unroll
  for (int i = 0; i < 4; ++i) A[i] = ld16<AUX_NT>(rkv, v0 ^ KVOFF(i), 0);
  __builtin_amdgcn_sched_barrier(0);
  if (wave == 0) STAMP(12);

  unsigned long long ep;
  Tag tg;
#define EPOCH_AND_BARRIER() do { const char* wsp_ = a.ws; asm volatile("" : "+s"(wsp_)); \
    ep = *(const __attribute__((address_space(4))) unsigned long long*)(wsp_ + WS_EPOCH) + 1ull; \
    tg.t0 = (unsigned)ep; tg.t1 = (unsigned)(ep >> 32) | 0x80000000u; \
    asm volatile("" :: "s"(tg.t0), "s"(tg.t1), "s"(a.wo), "s"(a.out)); wg_barrier(); } while (0)
  const rsrc_t ws = mk(a.ws);
  volatile LAS unsigned* flg = (volatile LAS unsigned*)(L + L_FLG);
  const unsigned rg = lane >> 4, c = lane & 15;
  const bool has_f2 = bid >= 213;
  const unsigned d = bid - 213;
  const unsigned woff = (rg << 13) | (k << 8) | (c << 4);
  const bool cons = s == 0 && (h & 1) == 0;
  const unsigned cid = b * 16 + (h >> 1);
  const unsigned ooff = (b * 512 + (h >> 3) * 128 + ((h >> 1) & 1) * 64 + (lane & 7) * 8 + ((h >> 2) & 1) * 4 + rg) * 2;
  u32x4 W8[8];

  if (!kw && k < 2 && bid < 214) {
    __builtin_amdgcn_s_setprio(3);
    const unsigned r8 = lane >> 3, c8 = lane & 7;
    const rsrc_t rx = mk(a.x);
    u32x4 X[2];
    X[0] = ld16<0>(rx, lane << 4, 0); X[1] = ld16<0>(rx, lane << 4, 1024);
    unsigned RS, par, dst; const char* tb;
    if (bid < 128) {
      const unsigned lc = (idx << 2) | (((x >> 1) ^ (idx >> 1)) & 1) << 1 | (x >> 2);
      par = (x ^ idx ^ (idx >> 3)) & 1;
      RS = 16384; tb = a.wq + (size_t)(k * 256 + par) * 8192 + lc * 128;
      dst = WS_QP + ((((lc >> 1) * 2 + (lane >> 5)) * 2 + par) * 64 + (r8 & 3) * 16 + (lc & 1) * 8 + c8) * 16;
    } else {
      const unsigned u = bid - 128, mat = u >= 43, j = u - mat * 43;
      RS = 5504; tb = (mat ? a.wu : a.wg) + (size_t)k * (128 * 5504) + j * 128;
      const unsigned e32 = j * 2 + (c8 >> 2);
      par = e32 >= 43;
      dst = WS_F1 + (((e32 - par * 43) * 2 + mat) * 64 + ((lane >> 5) + par * 2) * 16 + (c8 & 3) * 4 + (r8 & 3)) * 16;
    }
    u32x4 W[16];
    {
      const rsrc_t rt = mk(tb);
      const unsigned vo = r8 * 16 * RS + (c8 << 4);
#pragma unroll
      for (int i = 0; i < 16; ++i) {
        if (A_PACE && i == A_PACE) EPOCH_AND_BARRIER();
        if (A_PACE && i >= A_PACE && (i & 3) == 0) pace(t0, 32 + i * 2 + (int)k * 4);
        W[i] = ld16<AUX_NT>(rt, vo, i * RS);
        if (A_PACE && (i & 3) == 3) __builtin_amdgcn_sched_barrier(0);
      }
    }
    __builtin_amdgcn_sched_barrier(0);
    if (!A_PACE) EPOCH_AND_BARRIER();
    LAS unsigned char* XE = L + L_XE + k * 2048;
#pragma unroll
    for (int bb = 0; bb < 2; ++bb) {
      float e[8];
#pragma unroll
      for (int j = 0; j < 4; ++j) { e[2 * j] = blo(X[bb][j]); e[2 * j + 1] = bhi(X[bb][j]); }
      float sm = 0.f;
#pragma unroll
      for (int j = 0; j < 8; ++j) sm += e[j];
      const float mean = wave_sum(sm) * (1.0f / 512.0f);
      float sq = 0.f;
#pragma unroll
      for (int j = 0; j < 8; ++j) { const float dv = e[j] - mean; sq = fmaf(dv, dv, sq); }
      const float var = wave_sum(sq) * (1.0f / 512.0f);
      const float mb = rbf(mean);
      const float inv = 1.0f / rbf(__builtin_amdgcn_sqrtf(rbf(rbf(var) + 1e-5f)));
#pragma unroll
      for (int j = 0; j < 8; ++j) e[j] = rbf(e[j] - mb) * inv;
      u32x2 ev = {pk(e[0], e[2]), pk(e[4], e[6])}, od = {pk(e[1], e[3]), pk(e[5], e[7])};
      *(LAS u32x2*)(XE + bb * 1024 + lane * 8) = ev;
      *(LAS u32x2*)(XE + bb * 1024 + 512 + lane * 8) = od;
    }
    asm volatile("s_waitcnt lgkmcnt(0)" ::: "memory");
    __builtin_amdgcn_sched_barrier(0);
    u32x4 xa[2][2];
    const unsigned xo = par * 512 + k * 256 + r8 * 32;
#pragma unroll
    for (int bb = 0; bb < 2; ++bb) { xa[bb][0] = *(LAS u32x4*)(XE + bb * 1024 + xo); xa[bb][1] = *(LAS u32x4*)(XE + bb * 1024 + xo + 16); }
    float acc[2][8];
#pragma unroll
    for (int j = 0; j < 8; ++j) { acc[0][j] = 0.f; acc[1][j] = 0.f; }
#pragma unroll
    for (int t = 0; t < 8; ++t)
#pragma unroll
      for (int j = 0; j < 4; ++j) {
        const unsigned lo = plo(W[2 * t][j], W[2 * t + 1][j]), hi = phi(W[2 * t][j], W[2 * t + 1][j]);
#pragma unroll
        for (int bb = 0; bb < 2; ++bb) {
          acc[bb][2 * j] = dot2(lo, xa[bb][t >> 2][t & 3], acc[bb][2 * j]);
          acc[bb][2 * j + 1] = dot2(hi, xa[bb][t >> 2][t & 3], acc[bb][2 * j + 1]);
        }
      }
    float u[8], w4[4];
#pragma unroll
    for (int j = 0; j < 8; ++j) u[j] = tr32(acc[0][j], acc[1][j]);
#pragma unroll
    for (int j = 0; j < 4; ++j) w4[j] = tr16(u[j], u[j + 4]);
    f32x2 z;
#pragma unroll
    for (int j = 0; j < 2; ++j) { const float t1 = w4[j] + dpp<ROR8>(w4[j]), t2 = w4[j + 2] + dpp<ROR8>(w4[j + 2]); z[j] = (lane & 8) ? t2 : t1; }
    if (k == 1) {
      *(LAS f32x2*)(L + L_ZB + lane * 8) = z;
      asm volatile("s_waitcnt lgkmcnt(0)" ::: "memory");
      if (lane == 0) flg[9] = 1u;
    } else {
      LDS_WAIT(flg[9] != 0u);
      z += *(LAS f32x2*)(L + L_ZB + lane * 8);
      put2(ws, dst, z.x, z.y, tg);
      STAMP(8);
    }
    __builtin_amdgcn_s_setprio(0);
  } else {
    if (kw && has_f2) {
      const rsrc_t rd = mk(a.wd + (size_t)d * 32768);
#pragma unroll
      for (int i = 0; i < 8; ++i) W8[i] = ld16<AUX_NT>(rd, woff, i << 10);
    }
    if (wave == 6) {
      if (lane < 16) flg[lane] = lane < 4 ? 0xffffffffu : 0u;
      *(LAS u32x4*)(L + L_FB + lane * 32) = u32x4{~0u, ~0u, ~0u, ~0u}; *(LAS u32x4*)(L + L_FB + lane * 32 + 16) = u32x4{~0u, ~0u, ~0u, ~0u};
      *(LAS u32x4*)(L + L_OB + lane * 32) = u32x4{~0u, ~0u, ~0u, ~0u}; *(LAS u32x4*)(L + L_OB + lane * 32 + 16) = u32x4{~0u, ~0u, ~0u, ~0u};
    }
    EPOCH_AND_BARRIER();
    if (wave == 7) {
      const char* pc = (const char*)(__builtin_amdgcn_s_getpc() & ~127ull);
      const unsigned v = __builtin_amdgcn_raw_buffer_load_b32(__builtin_amdgcn_make_buffer_rsrc((void*)pc, 0, CODE_LEN, 0x00020000), ((idx & 15) * 8 + (lane & 7)) * 128, 0, 0);
      asm volatile("" :: "v"(v));
    }
  }
  {
    const int pos0 = (kw ? 48 : 160) + 4 * (int)k;
#pragma unroll
    for (int g = 1; g < 8; ++g) {
      pace(t0, pos0 + g * 16);
#pragma unroll
      for (int i = 4 * g; i < 4 * g + 4; ++i) A[i] = ld16<AUX_NT>(rkv, v0 ^ KVOFF(i), 0);
      __builtin_amdgcn_sched_barrier(0);
    }
  }

  if (kw) {
    if (k == 0) STAMP(1);
    if (k == 0) {
      u32x4 g[2];
      gather2<2, 1024, 16>(ws, WS_QP + (h * 2 + b) * 2048 + lane * 16, tg, g);
      ((LAS unsigned*)(L + L_QB))[c * 4 + rg] = pk(asf(g[0].x) + asf(g[1].x), asf(g[0].y) + asf(g[1].y));
      asm volatile("s_waitcnt lgkmcnt(0)" ::: "memory");
      if (lane == 0) flg[5] = 1u;
    } else if (k == 1) {
      if (has_f2) {
        u32x4 g[2];
        gather2<2, 1024, 16>(ws, WS_F1 + d * 2048 + lane * 16, tg, g);
        const float g0 = rbf(tr32(asf(g[0].x), asf(g[0].x))), g1 = rbf(tr32(asf(g[0].y), asf(g[0].y)));
        const float u0 = rbf(tr32(asf(g[1].x), asf(g[1].x))), u1 = rbf(tr32(asf(g[1].y), asf(g[1].y)));
        if (lane < 32) ((LAS unsigned*)(L + L_HB))[lane] = pk(rbf(silu(g0)) * u0, rbf(silu(g1)) * u1);
        asm volatile("s_waitcnt lgkmcnt(0)" ::: "memory");
        if (lane == 0) flg[6] = 1u;
      }
    }
    LDS_WAIT(flg[5] != 0u);
    const u32x4 q = *(LAS u32x4*)(L + L_QB + c * 16);
    if (k == 0) STAMP(2);
    float xs[32];
#pragma unroll
    for (int i = 0; i < 32; ++i) xs[i] = dot2(A[i].w, q[3], dot2(A[i].z, q[2], dot2(A[i].y, q[1], dot2(A[i].x, q[0], 0.f))));
#define TR_STEP(HALF, CTRL, BIT) _Pragma("unroll") for (int i = 0; i < (HALF); ++i) { \
      const float t1_ = xs[i] + dpp<CTRL>(xs[i]), t2_ = xs[i + (HALF)] + dpp<CTRL>(xs[i + (HALF)]); xs[i] = (c & (BIT)) ? t2_ : t1_; }
    TR_STEP(16, ROR8, 8) TR_STEP(8, XOR7, 4) TR_STEP(4, XOR2, 2) TR_STEP(2, XOR1, 1)
#undef TR_STEP
    const float s0 = rbf(rbf(xs[0]) * 0.08838834764831845f), s1 = rbf(rbf(xs[1]) * 0.08838834764831845f);
    const float m = wave_max(fmaxf(s0, s1));
    const float p0 = __builtin_amdgcn_exp2f((s0 - m) * LOG2E), p1 = __builtin_amdgcn_exp2f((s1 - m) * LOG2E);
    ((LAS unsigned*)(L + L_PB))[k * 64 + lane] = pk(p0, p1);
    asm volatile("s_waitcnt lgkmcnt(0)" ::: "memory");
    if (lane == 0) flg[k] = asu(m);
    const float l = wave_sum(p0 + p1);
    if (lane == 0) put2(ws, WS_ML + (bh * 16 + s * 4 + k) * 16, m, l, tg);
    if (k == 0) STAMP(3);
    {
      unsigned v = 0;
      if (k == 1) { if (lane < 8) v = __builtin_amdgcn_raw_buffer_load_b32(ws, WS_OP + (bh * 4 + s) * 1024 + lane * 128, 0, AUX_SC1); }
      else if (k >= 2 && cons) v = __builtin_amdgcn_raw_buffer_load_b32(ws, WS_YP + cid * 16384 + ((k & 1) * 64 + lane) * 128, 0, AUX_SC1);
      asm volatile("" :: "v"(v));
    }
    const unsigned yline = 4 * k + ((lane >> 3) & 3), oob = (lane & 32) << 26;
    u32x4 sl = {SLICE(0), SLICE(1), SLICE(2), SLICE(3)};
    u32x4 B[8];
    if (has_f2) {
      to_frags(W8, B);
      LDS_WAIT(flg[6] != 0u);
#pragma unroll
      for (int bb = 0; bb < 2; ++bb)
        gemv32_put(B, *(LAS u32x4*)(L + L_HB + bb * 64 + rg * 16), ws, (WS_F2 + (((bb * 16 + yline) * 48 + d) * 8 + (lane & 7)) * 16) | oob, lane & 16, sl);
    }
    LDS_WAIT(flg[4] >= WO_GATE);
    u32x4 gml[1];
    gml[0] = ld16<AUX_SC1>(ws, WS_ML + (bh * 16 + c) * 16, 0);
    if (WO_DELAY) __builtin_amdgcn_s_sleep(WO_DELAY);
    __builtin_amdgcn_sched_barrier(0);
    {
      const rsrc_t ro = mk(a.wo + (size_t)(h * 128 + s * 32) * 1024);
#pragma unroll
      for (int i = 0; i < 8; ++i) {
        if (WO_STEP && i && (i & 1) == 0) __builtin_amdgcn_s_sleep(WO_STEP);
        W8[i] = ld16<AUX_NT>(ro, woff, i << 10);
        __builtin_amdgcn_sched_barrier(0);
      }
    }
    float wgt;
    {
      if (!none(bad2(gml[0], tg))) gather2<1, 0, 2>(ws, WS_ML + (bh * 16 + c) * 16, tg, gml);
      const float mi = asf(gml[0].x), li = asf(gml[0].y);
      float mq = fmaxf(mi, dpp<XOR1>(mi)); mq = fmaxf(mq, dpp<XOR2>(mq));
      float M = fmaxf(mq, dpp<XOR7>(mq)); M = fmaxf(M, dpp<ROR8>(M));
      const float Ls = row_sum(li * __builtin_amdgcn_exp2f((mi - M) * LOG2E));
      wgt = __builtin_amdgcn_exp2f((mq - M) * LOG2E) * __builtin_amdgcn_rcpf(Ls);
      asm volatile("" : "+v"(wgt));
      __builtin_amdgcn_sched_barrier(0);
    }
    if (k == 0) STAMP(4);
    to_frags(W8, B);
#pragma unroll
    for (int j = 0; j < 8; ++j) asm volatile("" : "+v"(B[j]));
    if (k == 3) STAMP(5);
    {
      unsigned yoff = (WS_YP + (((b * 16 + yline) * 128 + h * 4 + s) * 8 + (lane & 7)) * 16) | oob;
      unsigned long long upm = 0xffff0000ffff0000ull;
      asm volatile("" : "+v"(yoff), "+v"(sl), "+s"(upm));
      const bool up = __builtin_amdgcn_inverse_ballot_w64(upm);
      u32x4 g[1];
      gather2<1, 0, 0>(ws, WS_OP + (bh * 4 + s) * 1024 + ((c << 2) | rg) * 16, tg, g);
      if (k == 3) STAMP(6);
      float lo = asf(g[0].x) * wgt, hi = asf(g[0].y) * wgt;
      lo += dpp<ROR4>(lo); hi += dpp<ROR4>(hi);
      lo += dpp<ROR8>(lo); hi += dpp<ROR8>(hi);
      const unsigned op = pk(lo, hi);
      const u32x4 av = {dppu<0x00>(op), dppu<0x55>(op), dppu<0xAA>(op), dppu<0xFF>(op)};
      gemv32_put(B, av, ws, yoff, up, sl);
      if (lane == 0) flg[7] = 1u;
    }
    if (k == 3) STAMP(7);
  } else {
    if (lane == 0) __hip_atomic_fetch_add((LAS unsigned*)(L + L_FLG + 16), 1u, __ATOMIC_RELAXED, __HIP_MEMORY_SCOPE_WORKGROUP);
    if (k == 3) STAMP(9);
    u32x4 mk4;
    for (unsigned spin = 0;; ++spin) {
      mk4 = *(volatile LAS u32x4*)(L + L_FLG);
      if ((mk4.x != 0xffffffffu && mk4.y != 0xffffffffu && mk4.z != 0xffffffffu && mk4.w != 0xffffffffu) || spin > (SPIN_CAP << 4)) break;
      __builtin_amdgcn_s_sleep(1);
    }
    asm volatile("" ::: "memory");
    u32x4 pp[4];
#pragma unroll
    for (int i = 0; i < 4; ++i) pp[i] = *(LAS u32x4*)(L + L_PB + k * 256 + rg * 64 + i * 16);
    const float mown = asf(k == 0 ? mk4.x : k == 1 ? mk4.y : k == 2 ? mk4.z : mk4.w);
    float cf = __builtin_amdgcn_exp2f((mown - fmaxf(fmaxf(asf(mk4.x), asf(mk4.y)), fmaxf(asf(mk4.z), asf(mk4.w)))) * LOG2E);
    unsigned opoff = WS_OP + ((bh * 4 + (c >> 2)) * 64 + s * 16 + rg * 4 + (c & 3)) * 16;
    f32x4 acc[8];
#pragma unroll
    for (int j = 0; j < 8; ++j) acc[j] = f32x4{0.f, 0.f, 0.f, 0.f};
#pragma unroll
    for (int t = 0; t < 4; ++t) {
      u32x4 B[8];
#pragma unroll
      for (int u = 0; u < 4; ++u) {
#pragma unroll
        for (int j = 0; j < 4; ++j) { B[2 * j][u] = plo(A[8 * t + 2 * u][j], A[8 * t + 2 * u + 1][j]); B[2 * j + 1][u] = phi(A[8 * t + 2 * u][j], A[8 * t + 2 * u + 1][j]); }
        if (t == 3 && u == 1) {
#pragma unroll
          for (int j = 0; j < 8; ++j) asm volatile("" : "+v"(B[j]), "+v"(acc[j]));
          asm volatile("" : "+v"(cf), "+v"(opoff));
          __builtin_amdgcn_sched_barrier(0);
        }
      }
#pragma unroll
      for (int j = 0; j < 8; ++j) acc[j] = mfma(pp[t], B[j], acc[j]);
    }
    {
      const volatile LAS u32x2* ob = (const volatile LAS u32x2*)(L + L_OB + lane * 8);
      u32x2 o1 = ob[(k ^ 1) * 64], o2 = ob[(k ^ 2) * 64], o3 = ob[(k ^ 3) * 64];
      const bool m1 = lane & 16, m2 = lane & 32;
      const float ea = m2 ? acc[4][0] : acc[0][0], eb = m2 ? acc[6][0] : acc[2][0], fa = m2 ? acc[5][0] : acc[1][0], fb = m2 ? acc[7][0] : acc[3][0];
      float r0 = (m1 ? eb : ea) * cf, r1 = (m1 ? fb : fa) * cf;
      asm volatile("" : "+v"(r0), "+v"(r1));
      bool all = nolane(max(max(o1.x, o2.x), o3.x) == 0xffffffffu);
      if (!all) {
        *(volatile LAS u32x2*)(L + L_OB + k * 512 + lane * 8) = u32x2{asu(r0), asu(r1)};
        o1 = ob[(k ^ 1) * 64]; o2 = ob[(k ^ 2) * 64]; o3 = ob[(k ^ 3) * 64];
        all = nolane(max(max(o1.x, o2.x), o3.x) == 0xffffffffu);
      }
      if (all) {
        put2(ws, opoff, (r0 + asf(o1.x)) + (asf(o2.x) + asf(o3.x)), (r1 + asf(o1.y)) + (asf(o2.y) + asf(o3.y)), tg);
        STAMP(11);
      }
    }
    if (cons) {
      if (k == 0) {
        const float xv = blo(__builtin_amdgcn_raw_buffer_load_b16(mk(a.x), ooff, 0, 0));
        u32x4 g[6];
        const bool v5 = lane < 24;
        const unsigned base = WS_F2 + cid * (48 * 128) + lane * 16;
        for (unsigned spin = 0;; ++spin) {
          asm volatile("" ::: "memory");
#pragma unroll
          for (int i = 0; i < 6; ++i) g[i] = ld16<AUX_SC1>(ws, base, i * 1024);
          unsigned bad = 0;
#pragma unroll
          for (int i = 0; i < 5; ++i) bad |= strip4(g[i], tg);
          const unsigned b5 = strip4(g[5], tg);
          bad |= v5 ? b5 : 0u;
          if (none(bad & 0xfffu) || spin > SPIN_CAP) break;
          __builtin_amdgcn_s_sleep(4);
        }
        float f[4];
#pragma unroll
        for (int j = 0; j < 4; ++j) f[j] = ((asf(g[0][j]) + asf(g[1][j])) + (asf(g[2][j]) + asf(g[3][j]))) + (asf(g[4][j]) + (v5 ? asf(g[5][j]) : 0.f));
        float ff = tr16(tr32(f[0], f[2]), tr32(f[1], f[3]));
        ff += dpp<ROR8>(ff);
        *(volatile LAS u32x2*)(L + L_ZB + lane * 8) = u32x2{asu(rbf(silu(rbf(ff)))), asu(xv)};
      }
      LDS_WAIT(flg[7] != 0u);
    }
  }
  if (cons) {
    const rsrc_t rout = mk(a.out);
    unsigned base = WS_YP + cid * 16384 + wave * 2048 + lane * 16, oo = ooff | ((lane & 8) << 28);
    unsigned fbo = lane * 32;
    asm volatile("" : "+v"(base), "+v"(oo), "+v"(fbo));
    u32x4 g[2];
    unsigned spin = 0, bad;
#pragma nounroll
    do {
      asm volatile("" ::: "memory");
#pragma unroll
      for (int i = 0; i < 2; ++i) g[i] = ld16<AUX_SC1>(ws, base, i * 1024);
      unsigned live = ++spin > SPIN_CAP ? 0u : 0xfffu;
      asm volatile("" : "+s"(live));
      bad = (strip4(g[0], tg) | strip4(g[1], tg)) & live;
    } while (!none(bad));
    if (wave == 4) STAMP(13);
    float y = tr16(tr32(asf(g[0].x) + asf(g[1].x), asf(g[0].z) + asf(g[1].z)), tr32(asf(g[0].y) + asf(g[1].y), asf(g[0].w) + asf(g[1].w)));
    y += dpp<ROR8>(y);
    *(volatile LAS unsigned*)(L + L_FB + fbo + wave * 4) = asu(y);
    const u32x4 ra = *(volatile LAS u32x4*)(L + L_FB + fbo), rb = *(volatile LAS u32x4*)(L + L_FB + fbo + 16);
    const u32x2 gx = *(volatile LAS u32x2*)(L + L_ZB + (fbo >> 2));
    if (nolane(max(max(max(ra.x, ra.y), max(ra.z, ra.w)), max(max(rb.x, rb.y), max(rb.z, rb.w))) == 0xffffffffu)) {
      y = ((asf(ra.x) + asf(ra.z)) + (asf(rb.x) + asf(rb.z))) + ((asf(ra.y) + asf(ra.w)) + (asf(rb.y) + asf(rb.w)));
      const unsigned o = pk(asf(gx.y) + rbf(rbf(y) * asf(gx.x)), 0.f);
      __builtin_amdgcn_raw_buffer_store_b16((unsigned short)o, rout, oo, 0, OUT_AUX);
      if (bid == 0 && lane == 0) { u32x2 e2 = {(unsigned)ep, (unsigned)(ep >> 32)}; __builtin_amdgcn_raw_buffer_store_b64(e2, ws, WS_EPOCH, 0, EP_AUX); }
      STAMP(14);
    }
  }
  FLUSH();
}

extern "C" void kernel_launch(void* const* d_in, const int* in_sizes, int n_in,
                              void* d_out, int out_size, void* d_ws, size_t ws_size,
                              hipStream_t stream) {
  Args a;
  a.x = (const char*)d_in[0]; a.kc = (const char*)d_in[1]; a.vc = (const char*)d_in[2];
  a.wq = (const char*)d_in[3]; a.wo = (const char*)d_in[4];
  a.wg = (const char*)d_in[6]; a.wu = (const char*)d_in[7]; a.wd = (const char*)d_in[8];
  a.out = (char*)d_out; a.ws = (char*)d_ws;
  fused_decode<<<256, 512, 0, stream>>>(a.kc, a.vc, a.x, a.wq, a.wg, a.wu, a.wd, a.ws, a.wo, a.out);
}
```
